```python
import torch

def _softmax(x, axis=-1):
    return torch.softmax(x, dim=axis)

def _gelu(x):
    return torch.nn.functional.gelu(x, approximate='tanh')

def _silu(x):
    return torch.nn.functional.silu(x)

def _sigmoid(x):
    return torch.sigmoid(x)

def _layernorm(x, axis=-1, eps=1e-05):
    m = x.mean(dim=axis, keepdim=True)
    v = x.var(dim=axis, keepdim=True, unbiased=False)
    return (x - m) / torch.sqrt(v + eps)

def _scatter_add(tgt, idx, src, axis=0):
    return tgt.clone().index_add_(axis, idx, src)

def _segment_sum(src, seg, n):
    out = torch.zeros((n,) + src.shape[1:], dtype=src.dtype)
    return out.index_add_(0, seg, src)

def _pad(x, pw):
    flat = tuple((v for pair in reversed(pw) for v in pair))
    return torch.nn.functional.pad(x, flat)
kernel_name = 'adaLN_cond__ssm_conv_h3__mixer_channel_mlp__prompt'

def setup_inputs():

    _w = lambda a: (lambda t: t.to(torch.bfloat16) if t.is_floating_point() else t)(torch.as_tensor(a))

    def _A_setup():
        B, S, D = (1, 2048, 640)
        ks = 4
        return {'q': _w((lambda *s: torch.randn(*s, dtype=torch.bfloat16) * 0.05)(B, S, D)), 'k': _w((lambda *s: torch.randn(*s, dtype=torch.bfloat16) * 0.8)(B, S, D)), 'v': _w((lambda *s: torch.randn(*s, dtype=torch.bfloat16) * 0.8)(B, S, D)), 'shift_k': _w((lambda *s: torch.randn(*s, dtype=torch.bfloat16) * 0.8)(D, ks) * 0.5), 'ssm_k': _w((lambda *s: torch.randn(*s, dtype=torch.bfloat16) * 0.8)(D, S) * (1.0 / S ** 0.5)), 'ssm_D': _w((lambda *s: torch.randn(*s, dtype=torch.bfloat16) * 0.8)(D))}
    _A_in = _A_setup()

    def _B_setup():
        B, S, D, F = (1, 2048, 640, 1728)
        return {'x': _w((lambda *s: torch.randn(*s, dtype=torch.bfloat16) * 0.05)(B, S, D)), 'w1': _w((lambda *s: torch.randn(*s, dtype=torch.bfloat16) * 3.2)(D, F) * D ** (-0.5)), 'b1': _w((lambda *s: torch.randn(*s, dtype=torch.bfloat16) * 1.6)(F)), 'w2': _w((lambda *s: torch.randn(*s, dtype=torch.bfloat16) * 1.6)(F, D) * F ** (-0.5)), 'b2': _w((lambda *s: torch.randn(*s, dtype=torch.bfloat16) * 3.2)(D))}
    _B_in = _B_setup()
    return {'A_q': _A_in['q'], 'A_k': _A_in['k'], 'A_v': _A_in['v'], 'A_shift_k': _A_in['shift_k'], 'A_ssm_k': _A_in['ssm_k'], 'A_ssm_D': _A_in['ssm_D'], 'B_x': _B_in['x'], 'B_w1': _B_in['w1'], 'B_b1': _B_in['b1'], 'B_w2': _B_in['w2'], 'B_b2': _B_in['b2'], 'c': _w((lambda *s: torch.randn(*s, dtype=torch.bfloat16) * 0.1)(1, 640) * 8.0), 'w_mod': _w((lambda *s: torch.randn(*s, dtype=torch.bfloat16) * 0.1)(640, 6 * 640) * 640 ** (-0.5) * 8.0)}

def reference(A_q, A_k, A_v, A_shift_k, A_ssm_k, A_ssm_D, B_x, B_w1, B_b1, B_w2, B_b2, c, w_mod):

    def _A_ref(q, k, v, shift_k, ssm_k, ssm_D):
        B, S, D = q.shape
        ks = shift_k.shape[-1]
        k_pad = _pad(k, ((0, 0), (ks - 1, 0), (0, 0)))
        k_shift = torch.zeros_like(k)
        for t in range(ks):
            k_shift = k_shift + k_pad[:, t:t + S, :] * shift_k[:, ks - 1 - t][None, None, :]
        kv = k_shift * v
        L = 2 * S
        kv_pad = _pad(kv, ((0, 0), (0, S), (0, 0)))
        kf_pad = _pad(ssm_k, ((0, 0), (0, S)))
        kv_f = (lambda x, axes=(-2, -1): torch.fft.rfft2(x.float(), dim=axes))(kv_pad[:, None, :, :], axes=(1, 2))
        kf_f = (lambda x, axes=(-2, -1): torch.fft.rfft2(x.float(), dim=axes))(kf_pad[:, None, :], axes=(1, 2))
        kf_f = (lambda x, *d: x.permute(*d))(kf_f, 1, 2, 0)
        y_f = kv_f * kf_f[None, :, :, :]
        y = (lambda x, s, axes=(-2, -1): torch.fft.irfft2(x, s=s, dim=axes).to(torch.bfloat16))(y_f, (1, L), axes=(1, 2))[:, 0, :S, :]
        y = y + kv * ssm_D[None, None, :]
        return q * y

    def _B_ref(x, w1, b1, w2, b2):
        y = _layernorm(x, axis=-1)
        h = _gelu(torch.einsum('bsd,df->bsf', y, w1) + b1)
        return torch.einsum('bsf,fd->bsd', h, w2) + b2
    x = A_q
    mod = (_silu(c) @ w_mod).reshape(c.shape[0], 6, 1, -1)
    sa, ca, ga, sb, cb, gb = (mod[:, 0].clone(), mod[:, 1].clone(), mod[:, 2].clone(), mod[:, 3].clone(), mod[:, 4].clone(), mod[:, 5].clone())
    h = _layernorm(x, axis=-1) * (1 + ca) + sa
    x = x + ga * _A_ref(h, A_k, A_v, A_shift_k, A_ssm_k, A_ssm_D)
    h = _layernorm(x, axis=-1) * (1 + cb) + sb
    x = x + gb * _B_ref(h, B_w1, B_b1, B_w2, B_b2)
    return x
```

```cpp
#include <hip/hip_runtime.h>
#include <hip/hip_bf16.h>

#define LAS __attribute__((address_space(3)))
#define GAS __attribute__((address_space(1)))
typedef unsigned short u16;
typedef unsigned v4u __attribute__((ext_vector_type(4)));
typedef unsigned v2u __attribute__((ext_vector_type(2)));
typedef short s16x4 __attribute__((ext_vector_type(4)));
typedef __bf16 bf16x8 __attribute__((ext_vector_type(8)));
typedef __bf16 bf16x2 __attribute__((ext_vector_type(2)));
typedef float f32x4 __attribute__((ext_vector_type(4)));
typedef float f32x2 __attribute__((ext_vector_type(2)));

constexpr int S_ = 2048, D_ = 640, F_ = 1728;
constexpr float LN_EPS = 1e-5f;

constexpr size_t WS_KVT = 0;
constexpr size_t WS_Y2T = WS_KVT + (size_t)D_ * S_ * 2;
constexpr size_t WS_X2  = WS_Y2T + (size_t)D_ * S_ * 2;
constexpr size_t WS_HT  = WS_X2 + (size_t)D_ * S_ * 2;
constexpr size_t WS_W1T = WS_HT + (size_t)S_ * F_ * 2;
constexpr size_t WS_W2T = WS_W1T + (size_t)D_ * F_ * 2;
constexpr size_t WS_MOD = WS_W2T + (size_t)D_ * F_ * 2;
constexpr size_t WS_STAMP = WS_MOD + 3840 * 4 + 1024;

#define STAMP(kid, slot) do {} while (0)
#define STAMP_ARG
#define STAMP_PASS

__device__ __forceinline__ float bf_lo(unsigned w) { return __uint_as_float(w << 16); }
__device__ __forceinline__ float bf_hi(unsigned w) { return __uint_as_float(w & 0xffff0000u); }
__device__ __forceinline__ unsigned pk_bf16(float lo, float hi) {
  f32x2 p = {lo, hi};
  return __builtin_bit_cast(unsigned, __builtin_convertvector(p, bf16x2));
}
__device__ __forceinline__ float rnd_bf16(float x) { return bf_lo(pk_bf16(x, 0.f)); }
__device__ __forceinline__ bf16x8 as_frag(v4u v) { return __builtin_bit_cast(bf16x8, v); }
__device__ __forceinline__ f32x4 mfma16(v4u a, v4u b, f32x4 c) {
  return __builtin_amdgcn_mfma_f32_16x16x32_bf16(as_frag(a), as_frag(b), c, 0, 0, 0);
}
__device__ __forceinline__ v2u tr_read(LAS char* p) {
  s16x4 r = __builtin_amdgcn_ds_read_tr16_b64_v4i16((LAS s16x4*)p);
  return __builtin_bit_cast(v2u, r);
}
__device__ __forceinline__ v4u tr_read2(LAS char* p0, LAS char* p1) {
  v2u a = tr_read(p0), b = tr_read(p1);
  return v4u{a.x, a.y, b.x, b.y};
}
__device__ __forceinline__ v4u ld_nt16(const void* p) { return __builtin_nontemporal_load((const v4u*)p); }
#define GLDS16(src, dst) __builtin_amdgcn_global_load_lds((const GAS unsigned*)(src), (LAS unsigned*)(dst), 16, 0, 0)
#define GLDS16_NT(src, dst) __builtin_amdgcn_global_load_lds((const GAS unsigned*)(src), (LAS unsigned*)(dst), 16, 0, 2)
#define WAIT_VM(n) asm volatile("s_waitcnt vmcnt(" #n ")" ::: "memory")
#define WAIT_LGKM0() asm volatile("s_waitcnt lgkmcnt(0)" ::: "memory")
#define BARRIER() __builtin_amdgcn_s_barrier()

__global__ __launch_bounds__(256) void k0_kv(const u16* __restrict__ k, const u16* __restrict__ v,
                                             const u16* __restrict__ shift_k, u16* __restrict__ kvT STAMP_ARG) {
  __shared__ __attribute__((aligned(16))) char lds_raw[(67 + 64) * 128];
  LAS char* kL = (LAS char*)lds_raw;
  LAS char* vL = kL + 67 * 128;
  STAMP(0, 0);
  const int tid = threadIdx.x;
  const int rt = blockIdx.x / 10, cb = blockIdx.x % 10;
  const int s0 = rt * 64, d0 = cb * 64;
  {
    const int c8 = tid & 7, r = tid >> 3;
    const size_t off0 = (size_t)(s0 + r) * D_ + d0 + c8 * 8, off1 = off0 + (size_t)32 * D_;
    v4u k0 = ld_nt16(k + off0), k1 = ld_nt16(k + off1);
    v4u v0 = ld_nt16(v + off0), v1 = ld_nt16(v + off1);
    v4u kh = {0u, 0u, 0u, 0u};
    if (tid < 24 && s0 > 0) kh = ld_nt16(k + (size_t)(s0 - 3 + r) * D_ + d0 + c8 * 8);
    auto sw = [&](int row) { return row * 128 + ((c8 ^ ((row >> 3) & 7)) << 4); };
    *(LAS v4u*)(kL + sw(r + 3)) = k0;
    *(LAS v4u*)(kL + sw(r + 35)) = k1;
    *(LAS v4u*)(vL + sw(r)) = v0;
    *(LAS v4u*)(vL + sw(r + 32)) = v1;
    if (tid < 24) *(LAS v4u*)(kL + sw(r)) = kh;
  }
  const int lane = tid & 63, wave = tid >> 6;
  const int rg = lane & 7, p = 8 * wave + (lane >> 3);
  const v4u wv = *(const v4u*)(shift_k + (size_t)(d0 + 2 * p) * 4);
  __syncthreads();
  const float wl[4] = {bf_lo(wv.x), bf_hi(wv.x), bf_lo(wv.y), bf_hi(wv.y)};
  const float wh[4] = {bf_lo(wv.z), bf_hi(wv.z), bf_lo(wv.w), bf_hi(wv.w)};
  float kl[11], kh[11];
#pragma unroll
  for (int i = 0; i < 11; ++i) {
    const int row = 8 * rg + i;
    unsigned w = *(LAS unsigned*)(kL + row * 128 + (((p >> 2) ^ ((row >> 3) & 7)) << 4) + (p & 3) * 4);
    kl[i] = bf_lo(w); kh[i] = bf_hi(w);
  }
  float ol[8], oh[8];
#pragma unroll
  for (int i = 0; i < 8; ++i) {
    const int row = 8 * rg + i;
    unsigned w = *(LAS unsigned*)(vL + row * 128 + (((p >> 2) ^ rg) << 4) + (p & 3) * 4);
    float sl = kl[i] * wl[3]; sl = fmaf(kl[i + 1], wl[2], sl); sl = fmaf(kl[i + 2], wl[1], sl); sl = fmaf(kl[i + 3], wl[0], sl);
    float sh = kh[i] * wh[3]; sh = fmaf(kh[i + 1], wh[2], sh); sh = fmaf(kh[i + 2], wh[1], sh); sh = fmaf(kh[i + 3], wh[0], sh);
    ol[i] = sl * bf_lo(w); oh[i] = sh * bf_hi(w);
  }
  v4u pl = {pk_bf16(ol[0], ol[1]), pk_bf16(ol[2], ol[3]), pk_bf16(ol[4], ol[5]), pk_bf16(ol[6], ol[7])};
  v4u ph = {pk_bf16(oh[0], oh[1]), pk_bf16(oh[2], oh[3]), pk_bf16(oh[4], oh[5]), pk_bf16(oh[6], oh[7])};
  u16* dst = kvT + (size_t)(d0 + 2 * p) * S_ + s0 + 8 * rg;
  *(v4u*)dst = pl;
  *(v4u*)(dst + S_) = ph;
  STAMP(0, 1);
}

constexpr int HC_STRIDE = 4160;
constexpr int U_STRIDE = 4576;
constexpr int K1_U_OFF = 4 * 4 * HC_STRIDE;
constexpr int K1_LDS = 140 * 1024;
constexpr int K1_CONV_WG = 160, K1_MOD_WG = 60, K1_TILE_WG = 36;

__device__ __forceinline__ void conv_body(LAS char* lds, const u16* __restrict__ kvT, const u16* __restrict__ ssm_k,
                                          const u16* __restrict__ ssm_D, u16* __restrict__ y2T, int wg) {
  const int tid = threadIdx.x, lane = tid & 63, wave = __builtin_amdgcn_readfirstlane(tid >> 6);
  const int d = 4 * wg + wave;
  LAS char* hc = lds + wave * (4 * HC_STRIDE);
  LAS char* uL = lds + K1_U_OFF + wave * U_STRIDE;
  const u16* hrow = ssm_k + (size_t)d * S_;
  v4u cur[4]; v2u prev[4];
#pragma unroll
  for (int it = 0; it < 4; ++it) {
    const int cI = lane + 64 * it;
    cur[it] = ld_nt16(hrow + 8 * cI);
    const int po = cI > 0 ? 8 * cI - 4 : 0;
    prev[it] = *(const v2u*)(hrow + po);
  }
#pragma unroll
  for (int j = 0; j < 4; ++j) GLDS16(kvT + (size_t)d * S_ + j * 512 + lane * 8, uL + 480 + j * 1024);
  const float Dd = bf_lo((unsigned)ssm_D[d]);
  const v4u z4 = {0u, 0u, 0u, 0u};
  if (lane < 30) *(LAS v4u*)(uL + lane * 16) = z4;
  if (lane < 8) *(LAS v4u*)(hc + (lane >> 1) * HC_STRIDE + (lane & 1) * 16) = z4;
#pragma unroll
  for (int it = 0; it < 4; ++it) {
    const int cI = lane + 64 * it;
    unsigned p0 = prev[it].x, p1 = prev[it].y;
    if (it == 0) { p0 = cI > 0 ? p0 : 0u; p1 = cI > 0 ? p1 : 0u; }
    const v4u c = cur[it];
    const unsigned om = __builtin_amdgcn_alignbit(p1, p0, 16);
    const unsigned o0 = __builtin_amdgcn_alignbit(c.x, p1, 16);
    const unsigned o1 = __builtin_amdgcn_alignbit(c.y, c.x, 16);
    const unsigned o2 = __builtin_amdgcn_alignbit(c.z, c.y, 16);
    const unsigned o3 = __builtin_amdgcn_alignbit(c.w, c.z, 16);
    LAS char* dst = hc + 32 + cI * 16;
    *(LAS v4u*)(dst) = c;
    *(LAS v4u*)(dst + HC_STRIDE) = v4u{o0, o1, o2, o3};
    *(LAS v4u*)(dst + 2 * HC_STRIDE) = v4u{p1, c.x, c.y, c.z};
    *(LAS v4u*)(dst + 3 * HC_STRIDE) = v4u{om, o0, o1, o2};
  }
  WAIT_VM(0);
  WAIT_LGKM0();
  const int g = lane >> 4, n = lane & 15, qq = n >> 2, p = n & 3;
  LAS char* aBase = hc + qq * HC_STRIDE + 64 - 16 * g + 8 * p - 8;
  LAS char* bBase = uL + 448 + 32 * n + 16 * g;
  f32x4 acc[8];
#pragma unroll
  for (int t = 0; t < 8; ++t) acc[t] = f32x4{0.f, 0.f, 0.f, 0.f};
  v4u A[2][8], B[2][8];
#pragma unroll
  for (int a = 0; a < 8; ++a) {
    A[0][a] = tr_read2(aBase + 512 * a + 8, aBase + 512 * a);
    B[0][a] = *(LAS v4u*)(bBase + 512 * a);
  }
#pragma unroll
  for (int rho = 0; rho < 8; ++rho) {
    const int cb = rho & 1, nb = cb ^ 1;
    if (rho < 7) {
#pragma unroll
      for (int a = 0; a < 8; ++a) {
        A[nb][a] = tr_read2(aBase + 64 * (rho + 1) + 512 * a + 8, aBase + 64 * (rho + 1) + 512 * a);
        B[nb][a] = *(LAS v4u*)(bBase - 64 * (rho + 1) + 512 * a);
      }
    }
#pragma unroll
    for (int b = 0; b < 8; ++b)
#pragma unroll
      for (int a = 0; a < 8 - b; ++a) acc[a + b] = mfma16(A[cb][a], B[cb][b], acc[a + b]);
  }
  u16* yrow = y2T + (size_t)d * S_ + 16 * n + 4 * g;
#pragma unroll
  for (int t = 0; t < 8; ++t) {
    const v2u u = *(LAS v2u*)(uL + 480 + 512 * t + 32 * n + 8 * g);
    const float y0 = fmaf(bf_lo(u.x), Dd, acc[t][0]), y1 = fmaf(bf_hi(u.x), Dd, acc[t][1]);
    const float y2 = fmaf(bf_lo(u.y), Dd, acc[t][2]), y3 = fmaf(bf_hi(u.y), Dd, acc[t][3]);
    *(v2u*)(yrow + 256 * t) = v2u{pk_bf16(y0, y1), pk_bf16(y2, y3)};
  }
}

__device__ __forceinline__ void mod_body(LAS char* lds, const u16* __restrict__ c, const u16* __restrict__ w_mod,
                                         float* __restrict__ mod, int wg) {
  const int tid = threadIdx.x;
  const int cc = tid & 7, rgp = tid >> 3;
  const u16* wp = w_mod + (size_t)rgp * 3840 + wg * 64 + cc * 8;
  v4u w[20];
#pragma unroll
  for (int i = 0; i < 20; ++i) w[i] = ld_nt16(wp + (size_t)i * 32 * 3840);
  float acc[8];
#pragma unroll
  for (int j = 0; j < 8; ++j) acc[j] = 0.f;
#pragma unroll
  for (int i = 0; i < 20; ++i) {
    const float cv = bf_lo((unsigned)c[rgp + 32 * i]);
    const float sv = rnd_bf16(cv / (1.f + __expf(-cv)));
    acc[0] = fmaf(sv, bf_lo(w[i].x), acc[0]); acc[1] = fmaf(sv, bf_hi(w[i].x), acc[1]);
    acc[2] = fmaf(sv, bf_lo(w[i].y), acc[2]); acc[3] = fmaf(sv, bf_hi(w[i].y), acc[3]);
    acc[4] = fmaf(sv, bf_lo(w[i].z), acc[4]); acc[5] = fmaf(sv, bf_hi(w[i].z), acc[5]);
    acc[6] = fmaf(sv, bf_lo(w[i].w), acc[6]); acc[7] = fmaf(sv, bf_hi(w[i].w), acc[7]);
  }
  LAS float* red = (LAS float*)lds;
  *(LAS f32x4*)(red + rgp * 64 + cc * 8) = f32x4{acc[0], acc[1], acc[2], acc[3]};
  *(LAS f32x4*)(red + rgp * 64 + cc * 8 + 4) = f32x4{acc[4], acc[5], acc[6], acc[7]};
  __syncthreads();
  if (tid < 64) {
    float s = 0.f;
#pragma unroll
    for (int r = 0; r < 32; ++r) s += red[r * 64 + tid];
    mod[wg * 64 + tid] = rnd_bf16(s);
  }
}

__device__ __forceinline__ void tile_body(LAS char* lds, const u16* __restrict__ w1, const u16* __restrict__ w2,
                                          u16* __restrict__ w1t, u16* __restrict__ w2t, int wg) {
  const int tid = threadIdx.x, lane = tid & 63, wave = __builtin_amdgcn_readfirstlane(tid >> 6);
  const int gw = wg * 4 + wave;
  LAS char* buf = lds + wave * (7 * 5120);
  constexpr int NITEM = 540 + 432;
#pragma unroll
  for (int i = 0; i < 7; ++i) {
    const int item = gw + 144 * i;
    LAS char* b = buf + i * 5120;
    if (item < 540) {
      const int kk = item / 27, fg = item % 27;
#pragma unroll
      for (int j = 0; j < 4; ++j)
        GLDS16(w1 + (size_t)(32 * kk + 8 * j + (lane >> 3)) * F_ + 64 * fg + (lane & 7) * 8, b + j * 1024);
    } else if (item < NITEM) {
      const int it2 = item - 540, kk = it2 >> 3, ct = it2 & 7;
#pragma unroll
      for (int j = 0; j < 5; ++j) {
        const int ch = 64 * j + lane, row = ch / 10, col = ch % 10;
        GLDS16(w2 + (size_t)(32 * kk + row) * D_ + 80 * ct + col * 8, b + j * 1024);
      }
    }
  }
  WAIT_VM(0);
  const int g = lane >> 4, q = (lane & 15) >> 2, p = lane & 3;
#pragma unroll
  for (int i = 0; i < 7; ++i) {
    const int item = gw + 144 * i;
    LAS char* b = buf + i * 5120;
    if (item < 540) {
      const int kk = item / 27, fg = item % 27;
#pragma unroll
      for (int PT = 0; PT < 4; ++PT) {
        const int Pl = PT >> 1, T = PT & 1;
        LAS char* a0 = b + (8 * g + q) * 128 + 2 * (32 * Pl + 8 * p + 4 * T);
        const v4u f = tr_read2(a0, a0 + 4 * 128);
        *(v4u*)(w1t + ((size_t)(((2 * fg + Pl) * 20 + kk) * 2 + T) * 64 + lane) * 8) = f;
      }
    } else if (item < NITEM) {
      const int it2 = item - 540, kk = it2 >> 3, ct = it2 & 7;
#pragma unroll
      for (int T5 = 0; T5 < 5; ++T5) {
        const int fo = T5 < 4 ? 32 * (T5 >> 1) + 8 * p + 4 * (T5 & 1) : 64 + 4 * p;
        LAS char* a0 = b + (8 * g + q) * 160 + 2 * fo;
        const v4u f = tr_read2(a0, a0 + 4 * 160);
        *(v4u*)(w2t + ((size_t)((ct * 54 + kk) * 5 + T5) * 64 + lane) * 8) = f;
      }
    }
  }
}

__global__ __launch_bounds__(256) void k1_conv(const u16* __restrict__ kvT, const u16* __restrict__ ssm_k,
                                               const u16* __restrict__ ssm_D, u16* __restrict__ y2T,
                                               const u16* __restrict__ c, const u16* __restrict__ w_mod, float* __restrict__ mod,
                                               const u16* __restrict__ w1, const u16* __restrict__ w2,
                                               u16* __restrict__ w1t, u16* __restrict__ w2t STAMP_ARG) {
  extern __shared__ __attribute__((aligned(16))) char lds_dyn[];
  LAS char* lds = (LAS char*)lds_dyn;
  STAMP(1, 0);
  const int b = blockIdx.x;
  if (b < K1_CONV_WG) conv_body(lds, kvT, ssm_k, ssm_D, y2T, b);
  else if (b < K1_CONV_WG + K1_MOD_WG) mod_body(lds, c, w_mod, mod, b - K1_CONV_WG);
  else tile_body(lds, w1, w2, w1t, w2t, b - K1_CONV_WG - K1_MOD_WG);
  STAMP(1, 1);
}

constexpr int K2_CST_OFF = 81920;
constexpr int K2_LDS = K2_CST_OFF + 4 * 640 * 4;

struct RowStat { float rs, nm; };

__device__ __forceinline__ RowStat row_stats(const v4u (&f)[10], int lane) {
  const v4u ones = {0x3f803f80u, 0x3f803f80u, 0x3f803f80u, 0x3f803f80u};
  f32x4 d1 = {0.f, 0.f, 0.f, 0.f}, d2 = {0.f, 0.f, 0.f, 0.f};
#pragma unroll
  for (int m = 0; m < 10; ++m) { d1 = mfma16(ones, f[m], d1); d2 = mfma16(f[m], f[m], d2); }
  const int n = lane & 15;
  float dg = (n & 2) ? ((n & 1) ? d2[3] : d2[2]) : ((n & 1) ? d2[1] : d2[0]);
  const int src = (n + 16 * (n >> 2)) * 4;
  float sq = __int_as_float(__builtin_amdgcn_ds_bpermute(src, __float_as_int(dg)));
  float sm = d1[0];
  sm += __int_as_float(__builtin_amdgcn_update_dpp(0, __float_as_int(sm), 0x128, 0xf, 0xf, false));
  sq += __int_as_float(__builtin_amdgcn_update_dpp(0, __float_as_int(sq), 0x128, 0xf, 0xf, false));
  const float mean = sm * (1.f / D_);
  const float var = fmaxf(sq * (1.f / D_) - mean * mean, 0.f);
  const float rs = __builtin_amdgcn_rsqf(var + LN_EPS);
  return RowStat{rs, -mean * rs};
}

__device__ __forceinline__ float gelu_tanh(float x) {
  constexpr float C0 = -2.f * 0.7978845608028654f * 1.4426950408889634f;
  constexpr float C1 = C0 * 0.044715f;
  const float e = __builtin_amdgcn_exp2f(x * fmaf(x * x, C1, C0));
  return x * __builtin_amdgcn_rcpf(1.f + e);
}

__global__ __launch_bounds__(512) void k2_ln_gemm1(const u16* __restrict__ x, const u16* __restrict__ y2T, const float* __restrict__ mod,
                                                   const u16* __restrict__ w1t, const u16* __restrict__ b1,
                                                   u16* __restrict__ x2g, u16* __restrict__ Ht STAMP_ARG) {
  extern __shared__ __attribute__((aligned(16))) char lds_dyn[];
  LAS char* lds = (LAS char*)lds_dyn;
  STAMP(2, 0);
  const int tid = threadIdx.x, lane = tid & 63, wave = __builtin_amdgcn_readfirstlane(tid >> 6);
  const int b = blockIdx.x, xl = b & 7, mI = b >> 3;
  const int rt = xl + 8 * (mI >> 3), cg8 = mI & 7;
  const int P0 = cg8 < 6 ? 7 * cg8 : 42 + 6 * (cg8 - 6);
  const int np = cg8 < 6 ? 7 : 6;
  const int r = lane & 7, cg = lane >> 3;
  const int row = 64 * rt + 8 * wave + r;
  v4u xw[10];
#pragma unroll
  for (int m = 0; m < 10; ++m) xw[m] = *(const v4u*)(x + (size_t)row * D_ + 8 * (8 * m + cg));
  {
    const int chl = lane >> 3, pos = lane & 7;
#pragma unroll
    for (int i = 0; i < 10; ++i) {
      const int I = wave * 10 + i, ch = 8 * I + chl;
      const int f = ((ch >> 1) & 1) | (((ch >> 3) & 1) << 1) | (((ch >> 4) & 1) << 2);
      GLDS16(y2T + (size_t)ch * S_ + 64 * rt + 8 * (pos ^ f), lds + I * 1024);
    }
  }
  LAS float* cst = (LAS float*)(lds + K2_CST_OFF);
  for (int i = tid; i < D_; i += 512) {
    const float sa = mod[i], ca = mod[D_ + i], ga = mod[2 * D_ + i], sb = mod[3 * D_ + i], cb = mod[4 * D_ + i];
    cst[i] = ga * rnd_bf16(1.f + ca);
    cst[D_ + i] = ga * sa;
    cst[2 * D_ + i] = rnd_bf16(1.f + cb);
    cst[3 * D_ + i] = sb;
  }
  WAIT_VM(0);
  __syncthreads();
  STAMP(2, 2);
  v4u yw[10];
  {
    const int G = lane >> 4, q = (lane & 15) >> 2, p = lane & 3;
#pragma unroll
    for (int m = 0; m < 10; ++m) {
      const int ch0 = 8 * (8 * m + 2 * G + (p >> 1)) + q, ch1 = ch0 + 4;
      const int f0 = ((ch0 >> 1) & 1) | (((ch0 >> 3) & 1) << 1) | (((ch0 >> 4) & 1) << 2);
      const int f1 = ((ch1 >> 1) & 1) | (((ch1 >> 3) & 1) << 1) | (((ch1 >> 4) & 1) << 2);
      yw[m] = tr_read2(lds + ch0 * 128 + ((wave ^ f0) << 4) + 8 * (p & 1), lds + ch1 * 128 + ((wave ^ f1) << 4) + 8 * (p & 1));
    }
  }
  const RowStat s1 = row_stats(xw, lane);
  float val[10][8];
  v4u pk[10];
#pragma unroll
  for (int m = 0; m < 10; ++m) {
    const int ch = 8 * (8 * m + cg);
    const f32x4 g1a = *(LAS f32x4*)(cst + ch), g1b = *(LAS f32x4*)(cst + ch + 4);
    const f32x4 g2a = *(LAS f32x4*)(cst + D_ + ch), g2b = *(LAS f32x4*)(cst + D_ + ch + 4);
#pragma unroll
    for (int j = 0; j < 8; ++j) {
      const unsigned xv = xw[m][j >> 1], yv = yw[m][j >> 1];
      const float xf = (j & 1) ? bf_hi(xv) : bf_lo(xv), yf = (j & 1) ? bf_hi(yv) : bf_lo(yv);
      const float G1 = j < 4 ? g1a[j & 3] : g1b[j & 3], G2 = j < 4 ? g2a[j & 3] : g2b[j & 3];
      const float u = fmaf(fmaf(xf, s1.rs, s1.nm), G1, G2);
      val[m][j] = fmaf(yf, u, xf);
    }
    pk[m] = v4u{pk_bf16(val[m][0], val[m][1]), pk_bf16(val[m][2], val[m][3]), pk_bf16(val[m][4], val[m][5]), pk_bf16(val[m][6], val[m][7])};
    const int chunk = 8 * m + cg;
    if (chunk >= 10 * cg8 && chunk < 10 * cg8 + 10) *(v4u*)(x2g + (size_t)row * D_ + 8 * chunk) = pk[m];
  }
  WAIT_LGKM0();
  BARRIER();
  const RowStat s2 = row_stats(pk, lane);
#pragma unroll
  for (int m = 0; m < 10; ++m) {
    const int ch = 8 * (8 * m + cg);
    const f32x4 a2a = *(LAS f32x4*)(cst + 2 * D_ + ch), a2b = *(LAS f32x4*)(cst + 2 * D_ + ch + 4);
    const f32x4 s2a = *(LAS f32x4*)(cst + 3 * D_ + ch), s2b = *(LAS f32x4*)(cst + 3 * D_ + ch + 4);
#pragma unroll
    for (int j = 0; j < 8; ++j) {
      const float A2 = j < 4 ? a2a[j & 3] : a2b[j & 3], S2 = j < 4 ? s2a[j & 3] : s2b[j & 3];
      val[m][j] = fmaf(fmaf(val[m][j], s2.rs, s2.nm), A2, S2);
    }
    pk[m] = v4u{pk_bf16(val[m][0], val[m][1]), pk_bf16(val[m][2], val[m][3]), pk_bf16(val[m][4], val[m][5]), pk_bf16(val[m][6], val[m][7])};
  }
  const RowStat s3 = row_stats(pk, lane);
  {
    const int mt = wave >> 1, n = 8 * (wave & 1) + r, g2 = cg & 3;
#pragma unroll
    for (int m = 0; m < 10; ++m) {
      float o[8];
#pragma unroll
      for (int j = 0; j < 8; ++j) o[j] = fmaf(val[m][j], s3.rs, s3.nm);
      const int kk = 2 * m + (cg >> 2);
      *(LAS v4u*)(lds + ((mt * 20 + kk) * 64 + n + 16 * g2) * 16) =
          v4u{pk_bf16(o[0], o[1]), pk_bf16(o[2], o[3]), pk_bf16(o[4], o[5]), pk_bf16(o[6], o[7])};
    }
  }
  WAIT_LGKM0();
  BARRIER();
  STAMP(2, 3);
  if (wave < np) {
    const int P = P0 + wave;
    const u16* wp = w1t + ((size_t)P * 40 * 64 + lane) * 8;
    f32x4 acc[4][2];
#pragma unroll
    for (int mt = 0; mt < 4; ++mt) { acc[mt][0] = f32x4{0.f, 0.f, 0.f, 0.f}; acc[mt][1] = f32x4{0.f, 0.f, 0.f, 0.f}; }
    constexpr int PF = 5;
    v4u wa[20][2];
#pragma unroll
    for (int kk = 0; kk < PF; ++kk) { wa[kk][0] = *(const v4u*)(wp + (kk * 2) * 512); wa[kk][1] = *(const v4u*)(wp + (kk * 2 + 1) * 512); }
    const v4u bias = *(const v4u*)(b1 + 32 * P + 8 * (lane >> 4));
#pragma unroll
    for (int kk = 0; kk < 20; ++kk) {
      if (kk + PF < 20) {
        wa[kk + PF][0] = *(const v4u*)(wp + ((kk + PF) * 2) * 512);
        wa[kk + PF][1] = *(const v4u*)(wp + ((kk + PF) * 2 + 1) * 512);
      }
#pragma unroll
      for (int mt = 0; mt < 4; ++mt) {
        const v4u bf = *(LAS v4u*)(lds + (mt * 20 + kk) * 1024 + lane * 16);
        acc[mt][0] = mfma16(wa[kk][0], bf, acc[mt][0]);
        acc[mt][1] = mfma16(wa[kk][1], bf, acc[mt][1]);
      }
    }
    STAMP(2, 4);
    const float bb[8] = {bf_lo(bias.x), bf_hi(bias.x), bf_lo(bias.y), bf_hi(bias.y), bf_lo(bias.z), bf_hi(bias.z), bf_lo(bias.w), bf_hi(bias.w)};
#pragma unroll
    for (int mt = 0; mt < 4; ++mt) {
      float h[8];
#pragma unroll
      for (int j = 0; j < 8; ++j) h[j] = gelu_tanh(acc[mt][j >> 2][j & 3] + bb[j]);
      *(v4u*)(Ht + (((size_t)(4 * rt + mt) * 54 + P) * 64 + lane) * 8) =
          v4u{pk_bf16(h[0], h[1]), pk_bf16(h[2], h[3]), pk_bf16(h[4], h[5]), pk_bf16(h[6], h[7])};
    }
  }
  STAMP(2, 1);
}

constexpr int K3_STAGE = 6 * 5 * 1024;
constexpr int K3_NS = 4;
constexpr int K3_LDS = K3_NS * K3_STAGE;

__global__ __launch_bounds__(320) void k3_gemm2(const u16* __restrict__ Ht, const u16* __restrict__ w2t, const u16* __restrict__ x2g,
                                                const float* __restrict__ mod, const u16* __restrict__ b2, u16* __restrict__ out STAMP_ARG) {
  extern __shared__ __attribute__((aligned(16))) char lds_dyn[];
  LAS char* lds = (LAS char*)lds_dyn;
  STAMP(3, 0);
  const int tid = threadIdx.x, lane = tid & 63, wave = __builtin_amdgcn_readfirstlane(tid >> 6);
  const int b = blockIdx.x, xl = b & 7, mI = b >> 3;
  const int rt = xl + 8 * (mI >> 3), ct = mI & 7;
  if (wave == 4) {
    const u16* src = w2t + ((size_t)ct * 54 * 5 * 64 + lane) * 8;
    auto issue = [&](int s) {
      LAS char* dst = lds + (s % K3_NS) * K3_STAGE;
#pragma unroll
      for (int i = 0; i < 30; ++i) GLDS16(src + (size_t)(s * 30 + i) * 512, dst + i * 1024);
    };
    issue(0); issue(1); issue(2);
#pragma unroll
    for (int s = 0; s < 9; ++s) {
      if (s < 7) WAIT_VM(60); else if (s == 7) WAIT_VM(30); else WAIT_VM(0);
      BARRIER();
      if (s + 3 < 9) issue(s + 3);
    }
  } else {
    const u16* hp = Ht + ((size_t)(4 * rt + wave) * 54 * 64 + lane) * 8;
    constexpr int PF = 12;
    v4u hb[54];
#pragma unroll
    for (int kk = 0; kk < PF; ++kk) hb[kk] = *(const v4u*)(hp + kk * 512);
    const int n = lane & 15, g = lane >> 4;
    const int row = 64 * rt + 16 * wave + n;
    const int f0 = 80 * ct + 8 * g, f2 = 80 * ct + 64 + 4 * g;
    const v4u xa = *(const v4u*)(x2g + (size_t)row * D_ + f0), xb = *(const v4u*)(x2g + (size_t)row * D_ + f0 + 32);
    const v2u xc = *(const v2u*)(x2g + (size_t)row * D_ + f2);
    f32x4 acc[5];
#pragma unroll
    for (int t = 0; t < 5; ++t) acc[t] = f32x4{0.f, 0.f, 0.f, 0.f};
#pragma unroll
    for (int s = 0; s < 9; ++s) {
      BARRIER();
      LAS char* slot = lds + (s % K3_NS) * K3_STAGE + lane * 16;
#pragma unroll
      for (int j = 0; j < 6; ++j) {
        const int kk = 6 * s + j;
        if (kk + PF < 54) hb[kk + PF] = *(const v4u*)(hp + (kk + PF) * 512);
#pragma unroll
        for (int t = 0; t < 5; ++t) {
          const v4u wa = *(LAS v4u*)(slot + (j * 5 + t) * 1024);
          acc[t] = mfma16(wa, hb[kk], acc[t]);
        }
      }
      WAIT_LGKM0();
    }
    STAMP(3, 2);
    const float* gb = mod + 5 * D_;
    const v4u ba = *(const v4u*)(b2 + f0), bbv = *(const v4u*)(b2 + f0 + 32);
    const v2u bc = *(const v2u*)(b2 + f2);
    float o[8];
    {
      const f32x4 g0 = *(const f32x4*)(gb + f0), g1 = *(const f32x4*)(gb + f0 + 4);
#pragma unroll
      for (int j = 0; j < 8; ++j) {
        const float xv = (j & 1) ? bf_hi(xa[j >> 1]) : bf_lo(xa[j >> 1]);
        const float bv = (j & 1) ? bf_hi(ba[j >> 1]) : bf_lo(ba[j >> 1]);
        o[j] = fmaf(j < 4 ? g0[j & 3] : g1[j & 3], acc[j >> 2][j & 3] + bv, xv);
      }
      *(v4u*)(out + (size_t)row * D_ + f0) = v4u{pk_bf16(o[0], o[1]), pk_bf16(o[2], o[3]), pk_bf16(o[4], o[5]), pk_bf16(o[6], o[7])};
    }
    {
      const f32x4 g0 = *(const f32x4*)(gb + f0 + 32), g1 = *(const f32x4*)(gb + f0 + 36);
#pragma unroll
      for (int j = 0; j < 8; ++j) {
        const float xv = (j & 1) ? bf_hi(xb[j >> 1]) : bf_lo(xb[j >> 1]);
        const float bv = (j & 1) ? bf_hi(bbv[j >> 1]) : bf_lo(bbv[j >> 1]);
        o[j] = fmaf(j < 4 ? g0[j & 3] : g1[j & 3], acc[2 + (j >> 2)][j & 3] + bv, xv);
      }
      *(v4u*)(out + (size_t)row * D_ + f0 + 32) = v4u{pk_bf16(o[0], o[1]), pk_bf16(o[2], o[3]), pk_bf16(o[4], o[5]), pk_bf16(o[6], o[7])};
    }
    {
      const f32x4 g0 = *(const f32x4*)(gb + f2);
#pragma unroll
      for (int j = 0; j < 4; ++j) {
        const float xv = (j & 1) ? bf_hi(xc[j >> 1]) : bf_lo(xc[j >> 1]);
        const float bv = (j & 1) ? bf_hi(bc[j >> 1]) : bf_lo(bc[j >> 1]);
        o[j] = fmaf(g0[j], acc[4][j] + bv, xv);
      }
      *(v2u*)(out + (size_t)row * D_ + f2) = v2u{pk_bf16(o[0], o[1]), pk_bf16(o[2], o[3])};
    }
  }
  STAMP(3, 1);
}

__global__ void empty_k(unsigned* p, int n) { if (n == 12345) p[threadIdx.x] = 1u; }
extern "C" void kernel_launch(void* const* d_in, const int* in_sizes, int n_in, void* d_out, int out_size,
                              void* d_ws, size_t ws_size, hipStream_t stream) {
  const u16* q = (const u16*)d_in[0];
  const u16* k = (const u16*)d_in[1];
  const u16* v = (const u16*)d_in[2];
  const u16* shift_k = (const u16*)d_in[3];
  const u16* ssm_k = (const u16*)d_in[4];
  const u16* ssm_D = (const u16*)d_in[5];
  const u16* w1 = (const u16*)d_in[7];
  const u16* b1 = (const u16*)d_in[8];
  const u16* w2 = (const u16*)d_in[9];
  const u16* b2 = (const u16*)d_in[10];
  const u16* c = (const u16*)d_in[11];
  const u16* w_mod = (const u16*)d_in[12];
  char* ws = (char*)d_ws;
  u16* kvT = (u16*)(ws + WS_KVT);
  u16* y2T = (u16*)(ws + WS_Y2T);
  u16* x2 = (u16*)(ws + WS_X2);
  u16* Ht = (u16*)(ws + WS_HT);
  u16* w1t = (u16*)(ws + WS_W1T);
  u16* w2t = (u16*)(ws + WS_W2T);
  float* mod = (float*)(ws + WS_MOD);
  (void)hipFuncSetAttribute((const void*)k1_conv, hipFuncAttributeMaxDynamicSharedMemorySize, K1_LDS);
  (void)hipFuncSetAttribute((const void*)k2_ln_gemm1, hipFuncAttributeMaxDynamicSharedMemorySize, K2_LDS);
  (void)hipFuncSetAttribute((const void*)k3_gemm2, hipFuncAttributeMaxDynamicSharedMemorySize, K3_LDS);
  k0_kv<<<320, 256, 0, stream>>>(k, v, shift_k, kvT STAMP_PASS);
  k1_conv<<<256, 256, K1_LDS, stream>>>(kvT, ssm_k, ssm_D, y2T, c, w_mod, mod, w1, w2, w1t, w2t STAMP_PASS);
  k2_ln_gemm1<<<256, 512, K2_LDS, stream>>>(q, y2T, mod, w1t, b1, x2, Ht STAMP_PASS);
  k3_gemm2<<<256, 320, K3_LDS, stream>>>(Ht, w2t, x2, mod, b2, (u16*)d_out STAMP_PASS);
}
```

```cpp
#include <hip/hip_runtime.h>
#include <hip/hip_bf16.h>
#include <type_traits>

#define LAS __attribute__((address_space(3)))
#define GAS __attribute__((address_space(1)))
typedef unsigned short u16;
typedef unsigned v4u __attribute__((ext_vector_type(4)));
typedef unsigned v2u __attribute__((ext_vector_type(2)));
typedef short s16x4 __attribute__((ext_vector_type(4)));
typedef __bf16 bf16x8 __attribute__((ext_vector_type(8)));
typedef __bf16 bf16x2 __attribute__((ext_vector_type(2)));
typedef float f32x4 __attribute__((ext_vector_type(4)));
typedef float f32x2 __attribute__((ext_vector_type(2)));

constexpr int S_ = 2048, D_ = 640, F_ = 1728;
constexpr float LN_EPS = 1e-5f;

constexpr size_t WS_KVT = 0;
constexpr size_t WS_Y2T = WS_KVT + (size_t)D_ * S_ * 2;
constexpr size_t WS_X2  = WS_Y2T + (size_t)D_ * S_ * 2;
constexpr size_t WS_HT  = WS_X2 + (size_t)D_ * S_ * 2;
constexpr size_t WS_W1T = WS_HT + (size_t)S_ * F_ * 2;
constexpr size_t WS_W2T = WS_W1T + (size_t)D_ * F_ * 2;
constexpr size_t WS_MOD = WS_W2T + (size_t)D_ * F_ * 2;
constexpr size_t WS_STAMP = WS_MOD + 3840 * 4 + 1024;

#define STAMP_DECL
#define STAMP(kid, slot) do {} while (0)
#define CSTAMP(kid, slot) do {} while (0)
#define STAMP_FLUSH(kid) do {} while (0)
#define STAMP_ARG
#define STAMP_PASS
#define STAMP_FWD
#define STAMP_PARAM

__device__ __forceinline__ float bf_lo(unsigned w) { return __uint_as_float(w << 16); }
__device__ __forceinline__ float bf_hi(unsigned w) { return __uint_as_float(w & 0xffff0000u); }
__device__ __forceinline__ unsigned pk_bf16(float lo, float hi) {
  f32x2 p = {lo, hi};
  return __builtin_bit_cast(unsigned, __builtin_convertvector(p, bf16x2));
}
__device__ __forceinline__ float rnd_bf16(float x) { return bf_lo(pk_bf16(x, 0.f)); }
__device__ __forceinline__ bf16x8 as_frag(v4u v) { return __builtin_bit_cast(bf16x8, v); }
__device__ __forceinline__ f32x4 mfma16(v4u a, v4u b, f32x4 c) {
  return __builtin_amdgcn_mfma_f32_16x16x32_bf16(as_frag(a), as_frag(b), c, 0, 0, 0);
}
__device__ __forceinline__ v2u tr_read(LAS char* p) {
  s16x4 r = __builtin_amdgcn_ds_read_tr16_b64_v4i16((LAS s16x4*)p);
  return __builtin_bit_cast(v2u, r);
}
__device__ __forceinline__ v4u tr_read2(LAS char* p0, LAS char* p1) {
  v2u a = tr_read(p0), b = tr_read(p1);
  return v4u{a.x, a.y, b.x, b.y};
}
__device__ __forceinline__ v4u ld_nt16(const void* p) { return __builtin_nontemporal_load((const v4u*)p); }
#define GLDS16(src, dst) __builtin_amdgcn_global_load_lds((const GAS unsigned*)(src), (LAS unsigned*)(dst), 16, 0, 0)
#define GLDS16_NT(src, dst) __builtin_amdgcn_global_load_lds((const GAS unsigned*)(src), (LAS unsigned*)(dst), 16, 0, 2)
#define WAIT_VM(n) asm volatile("s_waitcnt vmcnt(" #n ")" ::: "memory")
#define WAIT_LGKM0() asm volatile("s_waitcnt lgkmcnt(0)" ::: "memory")
#define BARRIER() __builtin_amdgcn_s_barrier()
#define SCHED_FENCE() __builtin_amdgcn_sched_barrier(0)
#define SGB(mask, n) __builtin_amdgcn_sched_group_barrier(mask, n, 0)
constexpr int SG_MFMA = 0x008, SG_VMEM_R = 0x020, SG_DS_R = 0x100;
__device__ __forceinline__ void glds16_asm(const void* gsrc, unsigned lds_addr) {
  asm volatile("s_mov_b32 m0, %1\n\ts_nop 0\n\tglobal_load_lds_dwordx4 %0, off" :: "v"(gsrc), "s"(lds_addr) : "memory");
}
__device__ __forceinline__ unsigned lds_addr_of(LAS char* p) { return (unsigned)(size_t)p; }

__global__ __launch_bounds__(256) void k0_kv(const u16* __restrict__ k, const u16* __restrict__ v,
                                             const u16* __restrict__ shift_k, u16* __restrict__ kvT STAMP_ARG) {
  __shared__ __attribute__((aligned(16))) char lds_raw[(67 + 64) * 128];
  LAS char* kL = (LAS char*)lds_raw;
  LAS char* vL = kL + 67 * 128;
  STAMP_DECL
  STAMP(0, 0);
  const int tid = threadIdx.x;
  const int rt = blockIdx.x / 10, cb = blockIdx.x % 10;
  const int s0 = rt * 64, d0 = cb * 64;
  {
    const int c8 = tid & 7, r = tid >> 3;
    const size_t off0 = (size_t)(s0 + r) * D_ + d0 + c8 * 8, off1 = off0 + (size_t)32 * D_;
    v4u k0 = ld_nt16(k + off0), k1 = ld_nt16(k + off1);
    v4u v0 = ld_nt16(v + off0), v1 = ld_nt16(v + off1);
    v4u kh = {0u, 0u, 0u, 0u};
    if (tid < 24 && s0 > 0) kh = ld_nt16(k + (size_t)(s0 - 3 + r) * D_ + d0 + c8 * 8);
    auto sw = [&](int row) { return row * 128 + ((c8 ^ ((row >> 3) & 7)) << 4); };
    *(LAS v4u*)(kL + sw(r + 3)) = k0;
    *(LAS v4u*)(kL + sw(r + 35)) = k1;
    *(LAS v4u*)(vL + sw(r)) = v0;
    *(LAS v4u*)(vL + sw(r + 32)) = v1;
    if (tid < 24) *(LAS v4u*)(kL + sw(r)) = kh;
  }
  const int lane = tid & 63, wave = tid >> 6;
  const int rg = lane & 7, p = 8 * wave + (lane >> 3);
  const v4u wv = *(const v4u*)(shift_k + (size_t)(d0 + 2 * p) * 4);
  __syncthreads();
  const float wl[4] = {bf_lo(wv.x), bf_hi(wv.x), bf_lo(wv.y), bf_hi(wv.y)};
  const float wh[4] = {bf_lo(wv.z), bf_hi(wv.z), bf_lo(wv.w), bf_hi(wv.w)};
  float kl[11], kh[11];
#pragma unroll
  for (int i = 0; i < 11; ++i) {
    const int row = 8 * rg + i;
    unsigned w = *(LAS unsigned*)(kL + row * 128 + (((p >> 2) ^ ((row >> 3) & 7)) << 4) + (p & 3) * 4);
    kl[i] = bf_lo(w); kh[i] = bf_hi(w);
  }
  float ol[8], oh[8];
#pragma unroll
  for (int i = 0; i < 8; ++i) {
    const int row = 8 * rg + i;
    unsigned w = *(LAS unsigned*)(vL + row * 128 + (((p >> 2) ^ rg) << 4) + (p & 3) * 4);
    float sl = kl[i] * wl[3]; sl = fmaf(kl[i + 1], wl[2], sl); sl = fmaf(kl[i + 2], wl[1], sl); sl = fmaf(kl[i + 3], wl[0], sl);
    float sh = kh[i] * wh[3]; sh = fmaf(kh[i + 1], wh[2], sh); sh = fmaf(kh[i + 2], wh[1], sh); sh = fmaf(kh[i + 3], wh[0], sh);
    ol[i] = sl * bf_lo(w); oh[i] = sh * bf_hi(w);
  }
  v4u pl = {pk_bf16(ol[0], ol[1]), pk_bf16(ol[2], ol[3]), pk_bf16(ol[4], ol[5]), pk_bf16(ol[6], ol[7])};
  v4u ph = {pk_bf16(oh[0], oh[1]), pk_bf16(oh[2], oh[3]), pk_bf16(oh[4], oh[5]), pk_bf16(oh[6], oh[7])};
  u16* dst = kvT + (size_t)(d0 + 2 * p) * S_ + s0 + 8 * rg;
  *(v4u*)dst = pl;
  *(v4u*)(dst + S_) = ph;
  STAMP(0, 1);
  STAMP_FLUSH(0);
}

constexpr int HC_STRIDE = 4160;
constexpr int U_STRIDE = 4576;
constexpr int K1_U_OFF = 4 * 4 * HC_STRIDE;
constexpr int K1_LDS = 140 * 1024;
constexpr int K1_CONV_WG = 160, K1_MOD_WG = 60, K1_TILE_WG = 36;

__device__ __forceinline__ void conv_body(LAS char* lds, const u16* __restrict__ kvT, const u16* __restrict__ ssm_k,
                                          const u16* __restrict__ ssm_D, u16* __restrict__ y2T, int wg STAMP_PARAM) {
  const int tid = threadIdx.x, lane = tid & 63, wave = __builtin_amdgcn_readfirstlane(tid >> 6);
  const int d = 4 * wg + wave;
  LAS char* hc = lds + wave * (4 * HC_STRIDE);
  LAS char* uL = lds + K1_U_OFF + wave * U_STRIDE;
  const u16* hrow = ssm_k + (size_t)d * S_;
  v4u cur[4]; v2u prev[4];
#pragma unroll
  for (int it = 0; it < 4; ++it) {
    const int cI = lane + 64 * it;
    cur[it] = ld_nt16(hrow + 8 * cI);
    const int po = cI > 0 ? 8 * cI - 4 : 0;
    prev[it] = *(const v2u*)(hrow + po);
  }
#pragma unroll
  for (int j = 0; j < 4; ++j) GLDS16(kvT + (size_t)d * S_ + j * 512 + lane * 8, uL + 480 + j * 1024);
  const float Dd = bf_lo((unsigned)ssm_D[d]);
  const v4u z4 = {0u, 0u, 0u, 0u};
  if (lane < 30) *(LAS v4u*)(uL + lane * 16) = z4;
  if (lane < 8) *(LAS v4u*)(hc + (lane >> 1) * HC_STRIDE + (lane & 1) * 16) = z4;
#pragma unroll
  for (int it = 0; it < 4; ++it) {
    const int cI = lane + 64 * it;
    unsigned p0 = prev[it].x, p1 = prev[it].y;
    if (it == 0) { p0 = cI > 0 ? p0 : 0u; p1 = cI > 0 ? p1 : 0u; }
    const v4u c = cur[it];
    const unsigned om = __builtin_amdgcn_alignbit(p1, p0, 16);
    const unsigned o0 = __builtin_amdgcn_alignbit(c.x, p1, 16);
    const unsigned o1 = __builtin_amdgcn_alignbit(c.y, c.x, 16);
    const unsigned o2 = __builtin_amdgcn_alignbit(c.z, c.y, 16);
    const unsigned o3 = __builtin_amdgcn_alignbit(c.w, c.z, 16);
    LAS char* dst = hc + 32 + cI * 16;
    *(LAS v4u*)(dst) = c;
    *(LAS v4u*)(dst + HC_STRIDE) = v4u{o0, o1, o2, o3};
    *(LAS v4u*)(dst + 2 * HC_STRIDE) = v4u{p1, c.x, c.y, c.z};
    *(LAS v4u*)(dst + 3 * HC_STRIDE) = v4u{om, o0, o1, o2};
  }
  WAIT_VM(0);
  WAIT_LGKM0();
  const int g = lane >> 4, n = lane & 15, qq = n >> 2, p = n & 3;
  LAS char* aBase = hc + qq * HC_STRIDE + 56 - 16 * g + 8 * p;
  LAS char* bBase = uL + 32 * n + 16 * g;
  f32x4 acc[8];
#pragma unroll
  for (int t = 0; t < 8; ++t) acc[t] = f32x4{0.f, 0.f, 0.f, 0.f};
  v4u A[2][8], B[2][8];
#pragma unroll
  for (int a = 0; a < 8; ++a) {
    B[0][a] = *(LAS v4u*)(bBase + 448 + 512 * a);
    A[0][a] = tr_read2(aBase + 512 * a + 8, aBase + 512 * a);
  }
  SCHED_FENCE();
  CSTAMP(1, 2);
#pragma unroll
  for (int rho = 0; rho < 8; ++rho) {
    const int cb = rho & 1, nb = cb ^ 1;
    if (rho < 7) {
#pragma unroll
      for (int a = 0; a < 8; ++a) {
        B[nb][a] = *(LAS v4u*)(bBase + 448 - 64 * (rho + 1) + 512 * a);
        A[nb][a] = tr_read2(aBase + 64 * (rho + 1) + 512 * a + 8, aBase + 64 * (rho + 1) + 512 * a);
      }
    }
#pragma unroll
    for (int b = 0; b < 8; ++b)
#pragma unroll
      for (int a = 0; a < 8 - b; ++a) acc[a + b] = mfma16(A[cb][a], B[cb][b], acc[a + b]);
    if (rho < 7) {
#pragma unroll
      for (int i = 0; i < 24; ++i) { SGB(SG_MFMA, 1); SGB(SG_DS_R, 1); }
      SGB(SG_MFMA, 12);
    }
    SCHED_FENCE();
  }
  CSTAMP(1, 3);
  u16* yrow = y2T + (size_t)d * S_ + 16 * n + 4 * g;
#pragma unroll
  for (int t = 0; t < 8; ++t) {
    const v2u u = *(LAS v2u*)(uL + 480 + 512 * t + 32 * n + 8 * g);
    const float y0 = fmaf(bf_lo(u.x), Dd, acc[t][0]), y1 = fmaf(bf_hi(u.x), Dd, acc[t][1]);
    const float y2 = fmaf(bf_lo(u.y), Dd, acc[t][2]), y3 = fmaf(bf_hi(u.y), Dd, acc[t][3]);
    *(v2u*)(yrow + 256 * t) = v2u{pk_bf16(y0, y1), pk_bf16(y2, y3)};
  }
}

__device__ __forceinline__ void mod_body(LAS char* lds, const u16* __restrict__ c, const u16* __restrict__ w_mod,
                                         float* __restrict__ mod, int wg) {
  const int tid = threadIdx.x;
  const int cc = tid & 7, rgp = tid >> 3;
  const u16* wp = w_mod + (size_t)rgp * 3840 + wg * 64 + cc * 8;
  v4u w[20];
#pragma unroll
  for (int i = 0; i < 20; ++i) w[i] = ld_nt16(wp + (size_t)i * 32 * 3840);
  float acc[8];
#pragma unroll
  for (int j = 0; j < 8; ++j) acc[j] = 0.f;
#pragma unroll
  for (int i = 0; i < 20; ++i) {
    const float cv = bf_lo((unsigned)c[rgp + 32 * i]);
    const float sv = rnd_bf16(cv / (1.f + __expf(-cv)));
    acc[0] = fmaf(sv, bf_lo(w[i].x), acc[0]); acc[1] = fmaf(sv, bf_hi(w[i].x), acc[1]);
    acc[2] = fmaf(sv, bf_lo(w[i].y), acc[2]); acc[3] = fmaf(sv, bf_hi(w[i].y), acc[3]);
    acc[4] = fmaf(sv, bf_lo(w[i].z), acc[4]); acc[5] = fmaf(sv, bf_hi(w[i].z), acc[5]);
    acc[6] = fmaf(sv, bf_lo(w[i].w), acc[6]); acc[7] = fmaf(sv, bf_hi(w[i].w), acc[7]);
  }
  LAS float* red = (LAS float*)lds;
  *(LAS f32x4*)(red + rgp * 64 + cc * 8) = f32x4{acc[0], acc[1], acc[2], acc[3]};
  *(LAS f32x4*)(red + rgp * 64 + cc * 8 + 4) = f32x4{acc[4], acc[5], acc[6], acc[7]};
  __syncthreads();
  if (tid < 64) {
    float s = 0.f;
#pragma unroll
    for (int r = 0; r < 32; ++r) s += red[r * 64 + tid];
    mod[wg * 64 + tid] = rnd_bf16(s);
  }
}

__device__ __forceinline__ void tile_body(LAS char* lds, const u16* __restrict__ w1, const u16* __restrict__ w2,
                                          u16* __restrict__ w1t, u16* __restrict__ w2t, int wg) {
  const int tid = threadIdx.x, lane = tid & 63, wave = __builtin_amdgcn_readfirstlane(tid >> 6);
  const int gw = wg * 4 + wave;
  LAS char* buf = lds + wave * (7 * 5120);
  constexpr int NITEM = 540 + 432;
#pragma unroll
  for (int i = 0; i < 7; ++i) {
    const int item = gw + 144 * i;
    LAS char* b = buf + i * 5120;
    if (item < 540) {
      const int kk = item / 27, fg = item % 27;
#pragma unroll
      for (int j = 0; j < 4; ++j)
        GLDS16(w1 + (size_t)(32 * kk + 8 * j + (lane >> 3)) * F_ + 64 * fg + (lane & 7) * 8, b + j * 1024);
    } else if (item < NITEM) {
      const int it2 = item - 540, kk = it2 >> 3, ct = it2 & 7;
#pragma unroll
      for (int j = 0; j < 5; ++j) {
        const int ch = 64 * j + lane, row = ch / 10, col = ch % 10;
        GLDS16(w2 + (size_t)(32 * kk + row) * D_ + 80 * ct + col * 8, b + j * 1024);
      }
    }
  }
  WAIT_VM(0);
  const int g = lane >> 4, q = (lane & 15) >> 2, p = lane & 3;
#pragma unroll
  for (int i = 0; i < 7; ++i) {
    const int item = gw + 144 * i;
    LAS char* b = buf + i * 5120;
    if (item < 540) {
      const int kk = item / 27, fg = item % 27;
#pragma unroll
      for (int PT = 0; PT < 4; ++PT) {
        const int Pl = PT >> 1, T = PT & 1;
        LAS char* a0 = b + (8 * g + q) * 128 + 2 * (32 * Pl + 8 * p + 4 * T);
        const v4u f = tr_read2(a0, a0 + 4 * 128);
        *(v4u*)(w1t + ((size_t)(((2 * fg + Pl) * 20 + kk) * 2 + T) * 64 + lane) * 8) = f;
      }
    } else if (item < NITEM) {
      const int it2 = item - 540, kk = it2 >> 3, ct = it2 & 7;
#pragma unroll
      for (int T5 = 0; T5 < 5; ++T5) {
        const int fo = T5 < 4 ? 32 * (T5 >> 1) + 8 * p + 4 * (T5 & 1) : 64 + 4 * p;
        LAS char* a0 = b + (8 * g + q) * 160 + 2 * fo;
        const v4u f = tr_read2(a0, a0 + 4 * 160);
        *(v4u*)(w2t + ((size_t)((ct * 54 + kk) * 5 + T5) * 64 + lane) * 8) = f;
      }
    }
  }
}

__global__ __launch_bounds__(256) void k1_conv(const u16* __restrict__ kvT, const u16* __restrict__ ssm_k,
                                               const u16* __restrict__ ssm_D, u16* __restrict__ y2T,
                                               const u16* __restrict__ c, const u16* __restrict__ w_mod, float* __restrict__ mod,
                                               const u16* __restrict__ w1, const u16* __restrict__ w2,
                                               u16* __restrict__ w1t, u16* __restrict__ w2t STAMP_ARG) {
  extern __shared__ __attribute__((aligned(16))) char lds_dyn[];
  LAS char* lds = (LAS char*)lds_dyn;
  STAMP_DECL
  STAMP(1, 0);
  const int b = blockIdx.x;
  if (b < K1_CONV_WG) conv_body(lds, kvT, ssm_k, ssm_D, y2T, b STAMP_FWD);
  else if (b < K1_CONV_WG + K1_MOD_WG) mod_body(lds, c, w_mod, mod, b - K1_CONV_WG);
  else tile_body(lds, w1, w2, w1t, w2t, b - K1_CONV_WG - K1_MOD_WG);
  STAMP(1, 1);
  STAMP_FLUSH(1);
}

constexpr int K2_CST_OFF = 81920;
constexpr int K2_LDS = K2_CST_OFF + 4 * 640 * 4;

struct RowStat { float rs, nm; };

__device__ __forceinline__ RowStat row_stats(const v4u (&f)[10], int lane) {
  const v4u ones = {0x3f803f80u, 0x3f803f80u, 0x3f803f80u, 0x3f803f80u};
  f32x4 d1 = {0.f, 0.f, 0.f, 0.f}, d2 = {0.f, 0.f, 0.f, 0.f};
#pragma unroll
  for (int m = 0; m < 10; ++m) { d1 = mfma16(ones, f[m], d1); d2 = mfma16(f[m], f[m], d2); }
  const int n = lane & 15;
  float dg = (n & 2) ? ((n & 1) ? d2[3] : d2[2]) : ((n & 1) ? d2[1] : d2[0]);
  const int src = (n + 16 * (n >> 2)) * 4;
  float sq = __int_as_float(__builtin_amdgcn_ds_bpermute(src, __float_as_int(dg)));
  float sm = d1[0];
  sm += __int_as_float(__builtin_amdgcn_update_dpp(0, __float_as_int(sm), 0x128, 0xf, 0xf, false));
  sq += __int_as_float(__builtin_amdgcn_update_dpp(0, __float_as_int(sq), 0x128, 0xf, 0xf, false));
  const float mean = sm * (1.f / D_);
  const float var = fmaxf(sq * (1.f / D_) - mean * mean, 0.f);
  const float rs = __builtin_amdgcn_rsqf(var + LN_EPS);
  return RowStat{rs, -mean * rs};
}

__device__ __forceinline__ float gelu_tanh(float x) {
  constexpr float C0 = -2.f * 0.7978845608028654f * 1.4426950408889634f;
  constexpr float C1 = C0 * 0.044715f;
  const float e = __builtin_amdgcn_exp2f(x * fmaf(x * x, C1, C0));
  return x * __builtin_amdgcn_rcpf(1.f + e);
}

#define GLD16(dst, ptr, off) asm volatile("global_load_dwordx4 %0, %1, off offset:" #off : "=v"(dst) : "v"(ptr) : "memory")
__device__ __forceinline__ void gld16(v4u& dst, const void* p) { asm volatile("global_load_dwordx4 %0, %1, off" : "=v"(dst) : "v"(p) : "memory"); }
template <int OFF> __device__ __forceinline__ void gld16o(v4u& dst, const void* p) {
  asm volatile("global_load_dwordx4 %0, %1, off offset:%c2" : "=v"(dst) : "v"(p), "i"(OFF) : "memory");
}
template <int N> __device__ __forceinline__ void wait_vm(v4u& a) { asm volatile("s_waitcnt vmcnt(%c1)" : "+v"(a) : "i"(N) : "memory"); }
template <int N> __device__ __forceinline__ void wait_vm(v4u& a, v4u& b) { asm volatile("s_waitcnt vmcnt(%c2)" : "+v"(a), "+v"(b) : "i"(N) : "memory"); }
__device__ __forceinline__ void tie(v4u& a) { asm volatile("" : "+v"(a)); }

template <int I, int N, class Fn> __device__ __forceinline__ void static_for(Fn&& f) {
  if constexpr (I < N) { f(std::integral_constant<int, I>{}); static_for<I + 1, N>(f); }
}

__global__ __launch_bounds__(512) void k2_ln_gemm1(const u16* __restrict__ x, const u16* __restrict__ y2T, const float* __restrict__ mod,
                                                   const u16* __restrict__ w1t, const u16* __restrict__ b1,
                                                   u16* __restrict__ x2g, u16* __restrict__ Ht STAMP_ARG) {
  extern __shared__ __attribute__((aligned(16))) char lds_dyn[];
  LAS char* lds = (LAS char*)lds_dyn;
  STAMP_DECL
  STAMP(2, 0);
  const int tid = threadIdx.x, lane = tid & 63, wave = __builtin_amdgcn_readfirstlane(tid >> 6);
  const int b = blockIdx.x, xl = b & 7, mI = b >> 3;
  const int rt = xl + 8 * (mI >> 3), cg8 = mI & 7;
  const int P0 = cg8 < 6 ? 7 * cg8 : 42 + 6 * (cg8 - 6);
  const int np = cg8 < 6 ? 7 : 6;
  const int r = lane & 7, cg = lane >> 3;
  const int row = 64 * rt + 8 * wave + r;
  v4u xw[10];
  {
    const u16* xp = x + (size_t)row * D_ + 8 * cg;
    static_for<0, 10>([&](auto m) { gld16o<128 * m>(xw[m], xp); });
  }
  v4u md[5];
  {
    const float* mp = mod + 4 * (tid < 160 ? tid : 159);
#pragma unroll
    for (int j = 0; j < 5; ++j) gld16(md[j], mp + j * D_);
  }
  {
    const int chl = lane >> 3, pos = lane & 7;
    const unsigned l0 = lds_addr_of(lds) + wave * 10240;
#pragma unroll
    for (int i = 0; i < 10; ++i) {
      const int I = wave * 10 + i, ch = 8 * I + chl;
      const int f = ((ch >> 1) & 1) | (((ch >> 3) & 1) << 1) | (((ch >> 4) & 1) << 2);
      glds16_asm(y2T + (size_t)ch * S_ + 64 * rt + 8 * (pos ^ f), l0 + i * 1024);
    }
  }
  asm volatile("s_waitcnt vmcnt(10)"
               : "+v"(xw[0]), "+v"(xw[1]), "+v"(xw[2]), "+v"(xw[3]), "+v"(xw[4]), "+v"(xw[5]), "+v"(xw[6]), "+v"(xw[7]), "+v"(xw[8]), "+v"(xw[9]),
                 "+v"(md[0]), "+v"(md[1]), "+v"(md[2]), "+v"(md[3]), "+v"(md[4]) :: "memory");
  LAS float* cst = (LAS float*)(lds + K2_CST_OFF);
  if (tid < 160) {
    const f32x4 sa = __builtin_bit_cast(f32x4, md[0]), ca = __builtin_bit_cast(f32x4, md[1]), ga = __builtin_bit_cast(f32x4, md[2]);
    const f32x4 sb = __builtin_bit_cast(f32x4, md[3]), cb = __builtin_bit_cast(f32x4, md[4]);
    f32x4 g1, a2;
#pragma unroll
    for (int j = 0; j < 4; ++j) { g1[j] = ga[j] * rnd_bf16(1.f + ca[j]); a2[j] = rnd_bf16(1.f + cb[j]); }
    *(LAS f32x4*)(cst + 4 * tid) = g1;
    *(LAS f32x4*)(cst + D_ + 4 * tid) = ga * sa;
    *(LAS f32x4*)(cst + 2 * D_ + 4 * tid) = a2;
    *(LAS f32x4*)(cst + 3 * D_ + 4 * tid) = sb;
  }
  const RowStat s1 = row_stats(xw, lane);
  WAIT_LGKM0();
  BARRIER();
  CSTAMP(2, 5);
  f32x2 val[10][4];
  LAS float* cl = cst + 8 * cg;
  {
    f32x4 c0[2][2], c1[2][2];
    c0[0][0] = *(LAS f32x4*)(cl); c0[0][1] = *(LAS f32x4*)(cl + 4);
    c1[0][0] = *(LAS f32x4*)(cl + D_); c1[0][1] = *(LAS f32x4*)(cl + D_ + 4);
    const f32x2 rs2 = {s1.rs, s1.rs}, nm2 = {s1.nm, s1.nm};
#pragma unroll
    for (int m = 0; m < 10; ++m) {
      const int cu = m & 1, nx = cu ^ 1;
      if (m < 9) {
        c0[nx][0] = *(LAS f32x4*)(cl + 64 * (m + 1)); c0[nx][1] = *(LAS f32x4*)(cl + 64 * (m + 1) + 4);
        c1[nx][0] = *(LAS f32x4*)(cl + D_ + 64 * (m + 1)); c1[nx][1] = *(LAS f32x4*)(cl + D_ + 64 * (m + 1) + 4);
      }
#pragma unroll
      for (int j = 0; j < 4; ++j) {
        const f32x2 xf = {bf_lo(xw[m][j]), bf_hi(xw[m][j])};
        const f32x2 G1 = {c0[cu][j >> 1][2 * (j & 1)], c0[cu][j >> 1][2 * (j & 1) + 1]};
        const f32x2 G2 = {c1[cu][j >> 1][2 * (j & 1)], c1[cu][j >> 1][2 * (j & 1) + 1]};
        val[m][j] = __builtin_elementwise_fma(__builtin_elementwise_fma(xf, rs2, nm2), G1, G2);
      }
      SCHED_FENCE();
    }
  }
  WAIT_VM(0);
  BARRIER();
  CSTAMP(2, 2);
  constexpr int PF = 5;
  const int P = P0 + (wave < np ? wave : np - 1);
  const u16* wp = w1t + ((size_t)P * 40 * 64 + lane) * 8;
  v4u wa[20][2];
  static_for<0, PF>([&](auto kk) { gld16(wa[kk][0], wp + (kk * 2) * 512); gld16(wa[kk][1], wp + (kk * 2 + 1) * 512); });
  v4u pk[10];
  {
    const int G = lane >> 4, q = (lane & 15) >> 2, p = lane & 3;
    const int chb = 8 * (2 * G + (p >> 1)) + q;
    const int fb = ((chb >> 1) & 1) | (((chb >> 3) & 1) << 1) | (((chb >> 4) & 1) << 2);
    const int fc = (((chb + 4) >> 1) & 1) | ((((chb + 4) >> 3) & 1) << 1) | ((((chb + 4) >> 4) & 1) << 2);
    LAS char* a0 = lds + chb * 128 + ((wave ^ fb) << 4) + 8 * (p & 1);
    LAS char* a1 = lds + (chb + 4) * 128 + ((wave ^ fc) << 4) + 8 * (p & 1);
    v4u yw[10];
    yw[0] = tr_read2(a0, a1);
    yw[1] = tr_read2(a0 + 8192, a1 + 8192);
#pragma unroll
    for (int m = 0; m < 10; ++m) {
      if (m + 2 < 10) yw[m + 2] = tr_read2(a0 + (m + 2) * 8192, a1 + (m + 2) * 8192);
#pragma unroll
      for (int j = 0; j < 4; ++j) {
        const f32x2 xf = {bf_lo(xw[m][j]), bf_hi(xw[m][j])};
        const f32x2 yf = {bf_lo(yw[m][j]), bf_hi(yw[m][j])};
        val[m][j] = __builtin_elementwise_fma(yf, val[m][j], xf);
        pk[m][j] = pk_bf16(val[m][j][0], val[m][j][1]);
      }
      SCHED_FENCE();
    }
  }
  if (cg8 >= 6) {
    u16* xp = x2g + (size_t)row * D_ + 8 * cg;
    if (cg8 == 6) {
#pragma unroll
      for (int m = 0; m < 5; ++m) *(v4u*)(xp + 64 * m) = pk[m];
    } else {
#pragma unroll
      for (int m = 5; m < 10; ++m) *(v4u*)(xp + 64 * m) = pk[m];
    }
  }
  WAIT_LGKM0();
  BARRIER();
  CSTAMP(2, 6);
  {
    f32x4 c0[2][2], c1[2][2];
    c0[0][0] = *(LAS f32x4*)(cl + 2 * D_); c0[0][1] = *(LAS f32x4*)(cl + 2 * D_ + 4);
    c1[0][0] = *(LAS f32x4*)(cl + 3 * D_); c1[0][1] = *(LAS f32x4*)(cl + 3 * D_ + 4);
    const RowStat s2 = row_stats(pk, lane);
    const f32x2 rs2 = {s2.rs, s2.rs}, nm2 = {s2.nm, s2.nm};
#pragma unroll
    for (int m = 0; m < 10; ++m) {
      const int cu = m & 1, nx = cu ^ 1;
      if (m < 9) {
        c0[nx][0] = *(LAS f32x4*)(cl + 2 * D_ + 64 * (m + 1)); c0[nx][1] = *(LAS f32x4*)(cl + 2 * D_ + 64 * (m + 1) + 4);
        c1[nx][0] = *(LAS f32x4*)(cl + 3 * D_ + 64 * (m + 1)); c1[nx][1] = *(LAS f32x4*)(cl + 3 * D_ + 64 * (m + 1) + 4);
      }
#pragma unroll
      for (int j = 0; j < 4; ++j) {
        const f32x2 A2 = {c0[cu][j >> 1][2 * (j & 1)], c0[cu][j >> 1][2 * (j & 1) + 1]};
        const f32x2 S2 = {c1[cu][j >> 1][2 * (j & 1)], c1[cu][j >> 1][2 * (j & 1) + 1]};
        val[m][j] = __builtin_elementwise_fma(__builtin_elementwise_fma(val[m][j], rs2, nm2), A2, S2);
        pk[m][j] = pk_bf16(val[m][j][0], val[m][j][1]);
      }
      SCHED_FENCE();
    }
  }
  {
    const RowStat s3 = row_stats(pk, lane);
    const f32x2 rs2 = {s3.rs, s3.rs}, nm2 = {s3.nm, s3.nm};
    const int mt = wave >> 1, n = 8 * (wave & 1) + r, g2 = cg & 3;
    LAS char* yb = lds + ((mt * 20 + (cg >> 2)) * 64 + n + 16 * g2) * 16;
#pragma unroll
    for (int m = 0; m < 10; ++m) {
      v4u o;
#pragma unroll
      for (int j = 0; j < 4; ++j) { const f32x2 t = __builtin_elementwise_fma(val[m][j], rs2, nm2); o[j] = pk_bf16(t[0], t[1]); }
      *(LAS v4u*)(yb + m * 2048) = o;
    }
  }
  WAIT_LGKM0();
  BARRIER();
  CSTAMP(2, 3);
  if (wave < np) {
    f32x4 acc[4][2];
#pragma unroll
    for (int mt = 0; mt < 4; ++mt) { acc[mt][0] = f32x4{0.f, 0.f, 0.f, 0.f}; acc[mt][1] = f32x4{0.f, 0.f, 0.f, 0.f}; }
    const v4u bias = *(const v4u*)(b1 + 32 * P + 8 * (lane >> 4));
    LAS char* bl = lds + lane * 16;
    v4u bf[2][4];
#pragma unroll
    for (int mt = 0; mt < 4; ++mt) bf[0][mt] = *(LAS v4u*)(bl + (mt * 20) * 1024);
    static_for<0, 20>([&](auto kc) {
      constexpr int kk = kc, cu = kk & 1, nx = cu ^ 1;
      if constexpr (kk + PF < 20) { gld16(wa[kk + PF][0], wp + ((kk + PF) * 2) * 512); gld16(wa[kk + PF][1], wp + ((kk + PF) * 2 + 1) * 512); }
      constexpr int younger = 2 * ((kk + PF < 20 ? kk + PF : 19) - kk);
      wait_vm<younger>(wa[kk][0], wa[kk][1]);
      if constexpr (kk < 19) {
#pragma unroll
        for (int mt = 0; mt < 4; ++mt) bf[nx][mt] = *(LAS v4u*)(bl + (mt * 20 + kk + 1) * 1024);
      }
#pragma unroll
      for (int mt = 0; mt < 4; ++mt) {
        acc[mt][0] = mfma16(wa[kk][0], bf[cu][mt], acc[mt][0]);
        acc[mt][1] = mfma16(wa[kk][1], bf[cu][mt], acc[mt][1]);
      }
      if constexpr (kk < 19) {
#pragma unroll
        for (int i = 0; i < 4; ++i) { SGB(SG_MFMA, 1); SGB(SG_DS_R, 1); }
        SGB(SG_MFMA, 4);
      }
      SCHED_FENCE();
    });
    CSTAMP(2, 4);
    const float bb[8] = {bf_lo(bias.x), bf_hi(bias.x), bf_lo(bias.y), bf_hi(bias.y), bf_lo(bias.z), bf_hi(bias.z), bf_lo(bias.w), bf_hi(bias.w)};
#pragma unroll
    for (int mt = 0; mt < 4; ++mt) {
      float h[8];
#pragma unroll
      for (int j = 0; j < 8; ++j) h[j] = gelu_tanh(acc[mt][j >> 2][j & 3] + bb[j]);
      *(v4u*)(Ht + (((size_t)(4 * rt + mt) * 54 + P) * 64 + lane) * 8) =
          v4u{pk_bf16(h[0], h[1]), pk_bf16(h[2], h[3]), pk_bf16(h[4], h[5]), pk_bf16(h[6], h[7])};
    }
  } else {
    WAIT_VM(0);
  }
  STAMP(2, 1);
  STAMP_FLUSH(2);
}

constexpr int K3_STAGE = 6 * 5 * 1024;
constexpr int K3_NS = 4;
constexpr int K3_LDS = K3_NS * K3_STAGE;

__device__ __forceinline__ void glds16x4(unsigned voff, const void* sbase, unsigned lds_addr) {
  asm volatile("s_mov_b32 m0, %2\n\ts_nop 0\n\t"
               "global_load_lds_dwordx4 %0, %1\n\tglobal_load_lds_dwordx4 %0, %1 offset:1024\n\t"
               "global_load_lds_dwordx4 %0, %1 offset:2048\n\tglobal_load_lds_dwordx4 %0, %1 offset:3072"
               :: "v"(voff), "s"(sbase), "s"(lds_addr) : "memory");
}
__device__ __forceinline__ void glds16x2(unsigned voff, const void* sbase, unsigned lds_addr) {
  asm volatile("s_mov_b32 m0, %2\n\ts_nop 0\n\t"
               "global_load_lds_dwordx4 %0, %1\n\tglobal_load_lds_dwordx4 %0, %1 offset:1024"
               :: "v"(voff), "s"(sbase), "s"(lds_addr) : "memory");
}

__global__ __launch_bounds__(320) void k3_gemm2(const u16* __restrict__ Ht, const u16* __restrict__ w2t, const u16* __restrict__ x2g,
                                                const float* __restrict__ mod, const u16* __restrict__ b2, u16* __restrict__ out STAMP_ARG) {
  extern __shared__ __attribute__((aligned(16))) char lds_dyn[];
  LAS char* lds = (LAS char*)lds_dyn;
  STAMP_DECL
  STAMP(3, 0);
  const int tid = threadIdx.x, lane = tid & 63, wave = __builtin_amdgcn_readfirstlane(tid >> 6);
  const int b = blockIdx.x, xl = b & 7, mI = b >> 3;
  const int rt = xl + 8 * (mI >> 3), ct = mI & 7;
  if (wave == 4) {
    const char* src = (const char*)w2t + (size_t)ct * 54 * 5 * 1024;
    const unsigned voff = lane * 16, l0 = lds_addr_of(lds);
    auto issue = [&](int s) {
      const char* sp = src + s * K3_STAGE;
      const unsigned dp = l0 + (s % K3_NS) * K3_STAGE;
#pragma unroll
      for (int i = 0; i < 7; ++i) glds16x4(voff, sp + i * 4096, dp + i * 4096);
      glds16x2(voff, sp + 7 * 4096, dp + 7 * 4096);
    };
    issue(0); issue(1);
#pragma unroll 1
    for (int s = 0; s < 7; ++s) {
      WAIT_VM(30);
      BARRIER();
      issue(s + 2);
    }
    WAIT_VM(30);
    BARRIER();
    WAIT_VM(0);
    BARRIER();
  } else {
    const u16* hp = Ht + ((size_t)(4 * rt + wave) * 54 * 64 + lane) * 8;
    constexpr int PF = 12;
    v4u hb[54];
    static_for<0, PF>([&](auto kk) { gld16(hb[kk], hp + kk * 512); });
    const int n = lane & 15, g = lane >> 4;
    const int row = 64 * rt + 16 * wave + n;
    const int f0 = 80 * ct + 8 * g, f2 = 80 * ct + 64 + 4 * g;
    f32x4 acc[5];
#pragma unroll
    for (int t = 0; t < 5; ++t) acc[t] = f32x4{0.f, 0.f, 0.f, 0.f};
    LAS char* lb = lds + lane * 16;
    v4u wa[2][5];
    BARRIER();
#pragma unroll
    for (int t = 0; t < 5; ++t) wa[0][t] = *(LAS v4u*)(lb + t * 1024);
    static_for<0, 54>([&](auto kc) {
      constexpr int kk = kc, cu = kk & 1, nx = cu ^ 1;
      if constexpr (kk + PF < 54) gld16(hb[kk + PF], hp + (kk + PF) * 512);
      constexpr int younger = (kk + PF < 54 ? kk + PF : 53) - kk;
      wait_vm<younger>(hb[kk]);
      if constexpr (kk % 6 == 5 && kk < 53) { WAIT_LGKM0(); BARRIER(); }
      if constexpr (kk < 53) {
        constexpr int s1 = (kk + 1) / 6, j1 = (kk + 1) % 6;
#pragma unroll
        for (int t = 0; t < 5; ++t) wa[nx][t] = *(LAS v4u*)(lb + (s1 % K3_NS) * K3_STAGE + (j1 * 5 + t) * 1024);
      }
#pragma unroll
      for (int t = 0; t < 5; ++t) acc[t] = mfma16(wa[cu][t], hb[kk], acc[t]);
      if constexpr (kk < 53) {
#pragma unroll
        for (int i = 0; i < 5; ++i) { SGB(SG_MFMA, 1); SGB(SG_DS_R, 1); }
      }
      SCHED_FENCE();
    });
    CSTAMP(3, 2);
    const v4u xa = *(const v4u*)(x2g + (size_t)row * D_ + f0), xb = *(const v4u*)(x2g + (size_t)row * D_ + f0 + 32);
    const v2u xc = *(const v2u*)(x2g + (size_t)row * D_ + f2);
    const float* gb = mod + 5 * D_;
    const v4u ba = *(const v4u*)(b2 + f0), bbv = *(const v4u*)(b2 + f0 + 32);
    const v2u bc = *(const v2u*)(b2 + f2);
    const f32x4 ga0 = *(const f32x4*)(gb + f0), ga1 = *(const f32x4*)(gb + f0 + 4);
    const f32x4 gb0 = *(const f32x4*)(gb + f0 + 32), gb1 = *(const f32x4*)(gb + f0 + 36);
    const f32x4 gc0 = *(const f32x4*)(gb + f2);
    float o[8];
#pragma unroll
    for (int j = 0; j < 8; ++j) {
      const float xv = (j & 1) ? bf_hi(xa[j >> 1]) : bf_lo(xa[j >> 1]);
      const float bv = (j & 1) ? bf_hi(ba[j >> 1]) : bf_lo(ba[j >> 1]);
      o[j] = fmaf(j < 4 ? ga0[j & 3] : ga1[j & 3], acc[j >> 2][j & 3] + bv, xv);
    }
    *(v4u*)(out + (size_t)row * D_ + f0) = v4u{pk_bf16(o[0], o[1]), pk_bf16(o[2], o[3]), pk_bf16(o[4], o[5]), pk_bf16(o[6], o[7])};
#pragma unroll
    for (int j = 0; j < 8; ++j) {
      const float xv = (j & 1) ? bf_hi(xb[j >> 1]) : bf_lo(xb[j >> 1]);
      const float bv = (j & 1) ? bf_hi(bbv[j >> 1]) : bf_lo(bbv[j >> 1]);
      o[j] = fmaf(j < 4 ? gb0[j & 3] : gb1[j & 3], acc[2 + (j >> 2)][j & 3] + bv, xv);
    }
    *(v4u*)(out + (size_t)row * D_ + f0 + 32) = v4u{pk_bf16(o[0], o[1]), pk_bf16(o[2], o[3]), pk_bf16(o[4], o[5]), pk_bf16(o[6], o[7])};
#pragma unroll
    for (int j = 0; j < 4; ++j) {
      const float xv = (j & 1) ? bf_hi(xc[j >> 1]) : bf_lo(xc[j >> 1]);
      const float bv = (j & 1) ? bf_hi(bc[j >> 1]) : bf_lo(bc[j >> 1]);
      o[j] = fmaf(gc0[j], acc[4][j] + bv, xv);
    }
    *(v2u*)(out + (size_t)row * D_ + f2) = v2u{pk_bf16(o[0], o[1]), pk_bf16(o[2], o[3])};
  }
  STAMP(3, 1);
  STAMP_FLUSH(3);
}

extern "C" void kernel_launch(void* const* d_in, const int* in_sizes, int n_in, void* d_out, int out_size,
                              void* d_ws, size_t ws_size, hipStream_t stream) {
  const u16* q = (const u16*)d_in[0];
  const u16* k = (const u16*)d_in[1];
  const u16* v = (const u16*)d_in[2];
  const u16* shift_k = (const u16*)d_in[3];
  const u16* ssm_k = (const u16*)d_in[4];
  const u16* ssm_D = (const u16*)d_in[5];
  const u16* w1 = (const u16*)d_in[7];
  const u16* b1 = (const u16*)d_in[8];
  const u16* w2 = (const u16*)d_in[9];
  const u16* b2 = (const u16*)d_in[10];
  const u16* c = (const u16*)d_in[11];
  const u16* w_mod = (const u16*)d_in[12];
  char* ws = (char*)d_ws;
  u16* kvT = (u16*)(ws + WS_KVT);
  u16* y2T = (u16*)(ws + WS_Y2T);
  u16* x2 = (u16*)(ws + WS_X2);
  u16* Ht = (u16*)(ws + WS_HT);
  u16* w1t = (u16*)(ws + WS_W1T);
  u16* w2t = (u16*)(ws + WS_W2T);
  float* mod = (float*)(ws + WS_MOD);
  (void)hipFuncSetAttribute((const void*)k1_conv, hipFuncAttributeMaxDynamicSharedMemorySize, K1_LDS);
  (void)hipFuncSetAttribute((const void*)k2_ln_gemm1, hipFuncAttributeMaxDynamicSharedMemorySize, K2_LDS);
  (void)hipFuncSetAttribute((const void*)k3_gemm2, hipFuncAttributeMaxDynamicSharedMemorySize, K3_LDS);
  k0_kv<<<320, 256, 0, stream>>>(k, v, shift_k, kvT STAMP_PASS);
  k1_conv<<<256, 256, K1_LDS, stream>>>(kvT, ssm_k, ssm_D, y2T, c, w_mod, mod, w1, w2, w1t, w2t STAMP_PASS);
  k2_ln_gemm1<<<256, 512, K2_LDS, stream>>>(q, y2T, mod, w1t, b1, x2, Ht STAMP_PASS);
  k3_gemm2<<<256, 320, K3_LDS, stream>>>(Ht, w2t, x2, mod, b2, (u16*)d_out STAMP_PASS);
}
```

```cpp
#include <hip/hip_runtime.h>
#include <hip/hip_bf16.h>
#include <type_traits>

#define LAS __attribute__((address_space(3)))
#define GAS __attribute__((address_space(1)))
typedef unsigned short u16;
typedef unsigned v4u __attribute__((ext_vector_type(4)));
typedef unsigned v2u __attribute__((ext_vector_type(2)));
typedef short s16x4 __attribute__((ext_vector_type(4)));
typedef __bf16 bf16x8 __attribute__((ext_vector_type(8)));
typedef __bf16 bf16x2 __attribute__((ext_vector_type(2)));
typedef float f32x4 __attribute__((ext_vector_type(4)));
typedef float f32x2 __attribute__((ext_vector_type(2)));

constexpr int S_ = 2048, D_ = 640, F_ = 1728;
constexpr float LN_EPS = 1e-5f;

constexpr size_t WS_KVT = 0;
constexpr size_t WS_Y2T = WS_KVT + (size_t)D_ * S_ * 2;
constexpr size_t WS_X2  = WS_Y2T + (size_t)D_ * S_ * 2;
constexpr size_t WS_HT  = WS_X2 + (size_t)D_ * S_ * 2;
constexpr size_t WS_W1T = WS_HT + (size_t)S_ * F_ * 2;
constexpr size_t WS_W2T = WS_W1T + (size_t)D_ * F_ * 2;
constexpr size_t WS_MOD = WS_W2T + (size_t)D_ * F_ * 2;
constexpr size_t WS_STAMP = WS_MOD + 3840 * 4 + 1024;

#define STAMP_DECL
#define STAMP(kid, slot) do {} while (0)
#define CSTAMP(kid, slot) do {} while (0)
#define STAMP_FLUSH(kid) do {} while (0)
#define STAMP_ARG
#define STAMP_PASS
#define STAMP_FWD
#define STAMP_PARAM

__device__ __forceinline__ float bf_lo(unsigned w) { return __uint_as_float(w << 16); }
__device__ __forceinline__ float bf_hi(unsigned w) { return __uint_as_float(w & 0xffff0000u); }
__device__ __forceinline__ unsigned pk_bf16(float lo, float hi) {
  f32x2 p = {lo, hi};
  return __builtin_bit_cast(unsigned, __builtin_convertvector(p, bf16x2));
}
__device__ __forceinline__ float rnd_bf16(float x) { return bf_lo(pk_bf16(x, 0.f)); }
__device__ __forceinline__ bf16x8 as_frag(v4u v) { return __builtin_bit_cast(bf16x8, v); }
__device__ __forceinline__ f32x4 mfma16(v4u a, v4u b, f32x4 c) {
  return __builtin_amdgcn_mfma_f32_16x16x32_bf16(as_frag(a), as_frag(b), c, 0, 0, 0);
}
__device__ __forceinline__ v2u tr_read(LAS char* p) {
  s16x4 r = __builtin_amdgcn_ds_read_tr16_b64_v4i16((LAS s16x4*)p);
  return __builtin_bit_cast(v2u, r);
}
__device__ __forceinline__ v4u tr_read2(LAS char* p0, LAS char* p1) {
  v2u a = tr_read(p0), b = tr_read(p1);
  return v4u{a.x, a.y, b.x, b.y};
}
__device__ __forceinline__ v4u ld_nt16(const void* p) { return __builtin_nontemporal_load((const v4u*)p); }
#define GLDS16(src, dst) __builtin_amdgcn_global_load_lds((const GAS unsigned*)(src), (LAS unsigned*)(dst), 16, 0, 0)
#define GLDS16_NT(src, dst) __builtin_amdgcn_global_load_lds((const GAS unsigned*)(src), (LAS unsigned*)(dst), 16, 0, 2)
#define WAIT_VM(n) asm volatile("s_waitcnt vmcnt(" #n ")" ::: "memory")
#define WAIT_LGKM0() asm volatile("s_waitcnt lgkmcnt(0)" ::: "memory")
#define BARRIER() __builtin_amdgcn_s_barrier()
#define SCHED_FENCE() __builtin_amdgcn_sched_barrier(0)
#define SGB(mask, n) __builtin_amdgcn_sched_group_barrier(mask, n, 0)
constexpr int SG_MFMA = 0x008, SG_VMEM_R = 0x020, SG_DS_R = 0x100;
__device__ __forceinline__ void glds16_asm(const void* gsrc, unsigned lds_addr) {
  asm volatile("s_mov_b32 m0, %1\n\ts_nop 0\n\tglobal_load_lds_dwordx4 %0, off" :: "v"(gsrc), "s"(lds_addr) : "memory");
}
__device__ __forceinline__ unsigned lds_addr_of(LAS char* p) { return (unsigned)(size_t)p; }

__global__ __launch_bounds__(256) void k0_kv(const u16* __restrict__ k, const u16* __restrict__ v,
                                             const u16* __restrict__ shift_k, u16* __restrict__ kvT STAMP_ARG) {
  __shared__ __attribute__((aligned(16))) char lds_raw[(67 + 64) * 128];
  LAS char* kL = (LAS char*)lds_raw;
  LAS char* vL = kL + 67 * 128;
  STAMP_DECL
  STAMP(0, 0);
  const int tid = threadIdx.x;
  const int rt = blockIdx.x / 10, cb = blockIdx.x % 10;
  const int s0 = rt * 64, d0 = cb * 64;
  {
    const int c8 = tid & 7, r = tid >> 3;
    const size_t off0 = (size_t)(s0 + r) * D_ + d0 + c8 * 8, off1 = off0 + (size_t)32 * D_;
    v4u k0 = ld_nt16(k + off0), k1 = ld_nt16(k + off1);
    v4u v0 = ld_nt16(v + off0), v1 = ld_nt16(v + off1);
    v4u kh = {0u, 0u, 0u, 0u};
    if (tid < 24 && s0 > 0) kh = ld_nt16(k + (size_t)(s0 - 3 + r) * D_ + d0 + c8 * 8);
    auto sw = [&](int row) { return row * 128 + ((c8 ^ ((row >> 3) & 7)) << 4); };
    *(LAS v4u*)(kL + sw(r + 3)) = k0;
    *(LAS v4u*)(kL + sw(r + 35)) = k1;
    *(LAS v4u*)(vL + sw(r)) = v0;
    *(LAS v4u*)(vL + sw(r + 32)) = v1;
    if (tid < 24) *(LAS v4u*)(kL + sw(r)) = kh;
  }
  const int lane = tid & 63, wave = tid >> 6;
  const int rg = lane & 7, p = 8 * wave + (lane >> 3);
  const v4u wv = *(const v4u*)(shift_k + (size_t)(d0 + 2 * p) * 4);
  __syncthreads();
  const float wl[4] = {bf_lo(wv.x), bf_hi(wv.x), bf_lo(wv.y), bf_hi(wv.y)};
  const float wh[4] = {bf_lo(wv.z), bf_hi(wv.z), bf_lo(wv.w), bf_hi(wv.w)};
  float kl[11], kh[11];
#pragma unroll
  for (int i = 0; i < 11; ++i) {
    const int row = 8 * rg + i;
    unsigned w = *(LAS unsigned*)(kL + row * 128 + (((p >> 2) ^ ((row >> 3) & 7)) << 4) + (p & 3) * 4);
    kl[i] = bf_lo(w); kh[i] = bf_hi(w);
  }
  float ol[8], oh[8];
#pragma unroll
  for (int i = 0; i < 8; ++i) {
    const int row = 8 * rg + i;
    unsigned w = *(LAS unsigned*)(vL + row * 128 + (((p >> 2) ^ rg) << 4) + (p & 3) * 4);
    float sl = kl[i] * wl[3]; sl = fmaf(kl[i + 1], wl[2], sl); sl = fmaf(kl[i + 2], wl[1], sl); sl = fmaf(kl[i + 3], wl[0], sl);
    float sh = kh[i] * wh[3]; sh = fmaf(kh[i + 1], wh[2], sh); sh = fmaf(kh[i + 2], wh[1], sh); sh = fmaf(kh[i + 3], wh[0], sh);
    ol[i] = sl * bf_lo(w); oh[i] = sh * bf_hi(w);
  }
  v4u pl = {pk_bf16(ol[0], ol[1]), pk_bf16(ol[2], ol[3]), pk_bf16(ol[4], ol[5]), pk_bf16(ol[6], ol[7])};
  v4u ph = {pk_bf16(oh[0], oh[1]), pk_bf16(oh[2], oh[3]), pk_bf16(oh[4], oh[5]), pk_bf16(oh[6], oh[7])};
  u16* dst = kvT + (size_t)(d0 + 2 * p) * S_ + s0 + 8 * rg;
  *(v4u*)dst = pl;
  *(v4u*)(dst + S_) = ph;
  STAMP(0, 1);
  STAMP_FLUSH(0);
}

constexpr int HC_STRIDE = 4160;
constexpr int U_STRIDE = 4576;
constexpr int K1_U_OFF = 4 * 4 * HC_STRIDE;
constexpr int K1_LDS = 140 * 1024;
constexpr int K1_CONV_WG = 160, K1_MOD_WG = 60, K1_TILE_WG = 36;

__device__ __forceinline__ void conv_body(LAS char* lds, const u16* __restrict__ kvT, const u16* __restrict__ ssm_k,
                                          const u16* __restrict__ ssm_D, u16* __restrict__ y2T, int wg STAMP_PARAM) {
  const int tid = threadIdx.x, lane = tid & 63, wave = __builtin_amdgcn_readfirstlane(tid >> 6);
  const int d = 4 * wg + wave;
  LAS char* hc = lds + wave * (4 * HC_STRIDE);
  LAS char* uL = lds + K1_U_OFF + wave * U_STRIDE;
  const u16* hrow = ssm_k + (size_t)d * S_;
  v4u cur[4]; v2u prev[4];
#pragma unroll
  for (int it = 0; it < 4; ++it) {
    const int cI = lane + 64 * it;
    cur[it] = ld_nt16(hrow + 8 * cI);
    const int po = cI > 0 ? 8 * cI - 4 : 0;
    prev[it] = *(const v2u*)(hrow + po);
  }
#pragma unroll
  for (int j = 0; j < 4; ++j) GLDS16(kvT + (size_t)d * S_ + j * 512 + lane * 8, uL + 480 + j * 1024);
  const float Dd = bf_lo((unsigned)ssm_D[d]);
  const v4u z4 = {0u, 0u, 0u, 0u};
  if (lane < 30) *(LAS v4u*)(uL + lane * 16) = z4;
  if (lane < 8) *(LAS v4u*)(hc + (lane >> 1) * HC_STRIDE + (lane & 1) * 16) = z4;
#pragma unroll
  for (int it = 0; it < 4; ++it) {
    const int cI = lane + 64 * it;
    unsigned p0 = prev[it].x, p1 = prev[it].y;
    if (it == 0) { p0 = cI > 0 ? p0 : 0u; p1 = cI > 0 ? p1 : 0u; }
    const v4u c = cur[it];
    const unsigned om = __builtin_amdgcn_alignbit(p1, p0, 16);
    const unsigned o0 = __builtin_amdgcn_alignbit(c.x, p1, 16);
    const unsigned o1 = __builtin_amdgcn_alignbit(c.y, c.x, 16);
    const unsigned o2 = __builtin_amdgcn_alignbit(c.z, c.y, 16);
    const unsigned o3 = __builtin_amdgcn_alignbit(c.w, c.z, 16);
    LAS char* dst = hc + 32 + cI * 16;
    *(LAS v4u*)(dst) = c;
    *(LAS v4u*)(dst + HC_STRIDE) = v4u{o0, o1, o2, o3};
    *(LAS v4u*)(dst + 2 * HC_STRIDE) = v4u{p1, c.x, c.y, c.z};
    *(LAS v4u*)(dst + 3 * HC_STRIDE) = v4u{om, o0, o1, o2};
  }
  WAIT_VM(0);
  WAIT_LGKM0();
  const int g = lane >> 4, n = lane & 15, qq = n >> 2, p = n & 3;
  LAS char* aBase = hc + qq * HC_STRIDE + 56 - 16 * g + 8 * p;
  LAS char* bBase = uL + 32 * n + 16 * g;
  f32x4 acc[8];
#pragma unroll
  for (int t = 0; t < 8; ++t) acc[t] = f32x4{0.f, 0.f, 0.f, 0.f};
  v4u A[2][8], B[2][8];
#pragma unroll
  for (int a = 0; a < 8; ++a) {
    B[0][a] = *(LAS v4u*)(bBase + 448 + 512 * a);
    A[0][a] = tr_read2(aBase + 512 * a + 8, aBase + 512 * a);
  }
  SCHED_FENCE();
  CSTAMP(1, 2);
#pragma unroll
  for (int rho = 0; rho < 8; ++rho) {
    const int cb = rho & 1, nb = cb ^ 1;
    if (rho < 7) {
#pragma unroll
      for (int a = 0; a < 8; ++a) {
        B[nb][a] = *(LAS v4u*)(bBase + 448 - 64 * (rho + 1) + 512 * a);
        A[nb][a] = tr_read2(aBase + 64 * (rho + 1) + 512 * a + 8, aBase + 64 * (rho + 1) + 512 * a);
      }
    }
#pragma unroll
    for (int b = 0; b < 8; ++b)
#pragma unroll
      for (int a = 0; a < 8 - b; ++a) acc[a + b] = mfma16(A[cb][a], B[cb][b], acc[a + b]);
    if (rho < 7) {
#pragma unroll
      for (int i = 0; i < 24; ++i) { SGB(SG_MFMA, 1); SGB(SG_DS_R, 1); }
      SGB(SG_MFMA, 12);
    }
    SCHED_FENCE();
  }
  CSTAMP(1, 3);
  u16* yrow = y2T + (size_t)d * S_ + 16 * n + 4 * g;
#pragma unroll
  for (int t = 0; t < 8; ++t) {
    const v2u u = *(LAS v2u*)(uL + 480 + 512 * t + 32 * n + 8 * g);
    const float y0 = fmaf(bf_lo(u.x), Dd, acc[t][0]), y1 = fmaf(bf_hi(u.x), Dd, acc[t][1]);
    const float y2 = fmaf(bf_lo(u.y), Dd, acc[t][2]), y3 = fmaf(bf_hi(u.y), Dd, acc[t][3]);
    *(v2u*)(yrow + 256 * t) = v2u{pk_bf16(y0, y1), pk_bf16(y2, y3)};
  }
}

__device__ __forceinline__ void mod_body(LAS char* lds, const u16* __restrict__ c, const u16* __restrict__ w_mod,
                                         float* __restrict__ mod, int wg) {
  const int tid = threadIdx.x;
  const int cc = tid & 7, rgp = tid >> 3;
  const u16* wp = w_mod + (size_t)rgp * 3840 + wg * 64 + cc * 8;
  v4u w[20];
#pragma unroll
  for (int i = 0; i < 20; ++i) w[i] = ld_nt16(wp + (size_t)i * 32 * 3840);
  float acc[8];
#pragma unroll
  for (int j = 0; j < 8; ++j) acc[j] = 0.f;
#pragma unroll
  for (int i = 0; i < 20; ++i) {
    const float cv = bf_lo((unsigned)c[rgp + 32 * i]);
    const float sv = rnd_bf16(cv / (1.f + __expf(-cv)));
    acc[0] = fmaf(sv, bf_lo(w[i].x), acc[0]); acc[1] = fmaf(sv, bf_hi(w[i].x), acc[1]);
    acc[2] = fmaf(sv, bf_lo(w[i].y), acc[2]); acc[3] = fmaf(sv, bf_hi(w[i].y), acc[3]);
    acc[4] = fmaf(sv, bf_lo(w[i].z), acc[4]); acc[5] = fmaf(sv, bf_hi(w[i].z), acc[5]);
    acc[6] = fmaf(sv, bf_lo(w[i].w), acc[6]); acc[7] = fmaf(sv, bf_hi(w[i].w), acc[7]);
  }
  LAS float* red = (LAS float*)lds;
  *(LAS f32x4*)(red + rgp * 64 + cc * 8) = f32x4{acc[0], acc[1], acc[2], acc[3]};
  *(LAS f32x4*)(red + rgp * 64 + cc * 8 + 4) = f32x4{acc[4], acc[5], acc[6], acc[7]};
  __syncthreads();
  if (tid < 64) {
    float s = 0.f;
#pragma unroll
    for (int r = 0; r < 32; ++r) s += red[r * 64 + tid];
    mod[wg * 64 + tid] = rnd_bf16(s);
  }
}

__device__ __forceinline__ void tile_body(LAS char* lds, const u16* __restrict__ w1, const u16* __restrict__ w2,
                                          u16* __restrict__ w1t, u16* __restrict__ w2t, int wg) {
  const int tid = threadIdx.x, lane = tid & 63, wave = __builtin_amdgcn_readfirstlane(tid >> 6);
  const int gw = wg * 4 + wave;
  LAS char* buf = lds + wave * (7 * 5120);
  constexpr int NITEM = 540 + 432;
#pragma unroll
  for (int i = 0; i < 7; ++i) {
    const int item = gw + 144 * i;
    LAS char* b = buf + i * 5120;
    if (item < 540) {
      const int kk = item / 27, fg = item % 27;
#pragma unroll
      for (int j = 0; j < 4; ++j)
        GLDS16(w1 + (size_t)(32 * kk + 8 * j + (lane >> 3)) * F_ + 64 * fg + (lane & 7) * 8, b + j * 1024);
    } else if (item < NITEM) {
      const int it2 = item - 540, kk = it2 >> 3, ct = it2 & 7;
#pragma unroll
      for (int j = 0; j < 5; ++j) {
        const int ch = 64 * j + lane, row = ch / 10, col = ch % 10;
        GLDS16(w2 + (size_t)(32 * kk + row) * D_ + 80 * ct + col * 8, b + j * 1024);
      }
    }
  }
  WAIT_VM(0);
  const int g = lane >> 4, q = (lane & 15) >> 2, p = lane & 3;
#pragma unroll
  for (int i = 0; i < 7; ++i) {
    const int item = gw + 144 * i;
    LAS char* b = buf + i * 5120;
    if (item < 540) {
      const int kk = item / 27, fg = item % 27;
#pragma unroll
      for (int PT = 0; PT < 4; ++PT) {
        const int Pl = PT >> 1, T = PT & 1;
        LAS char* a0 = b + (8 * g + q) * 128 + 2 * (32 * Pl + 8 * p + 4 * T);
        const v4u f = tr_read2(a0, a0 + 4 * 128);
        *(v4u*)(w1t + ((size_t)(((2 * fg + Pl) * 20 + kk) * 2 + T) * 64 + lane) * 8) = f;
      }
    } else if (item < NITEM) {
      const int it2 = item - 540, kk = it2 >> 3, ct = it2 & 7;
#pragma unroll
      for (int T5 = 0; T5 < 5; ++T5) {
        const int fo = T5 < 4 ? 32 * (T5 >> 1) + 8 * p + 4 * (T5 & 1) : 64 + 4 * p;
        LAS char* a0 = b + (8 * g + q) * 160 + 2 * fo;
        const v4u f = tr_read2(a0, a0 + 4 * 160);
        *(v4u*)(w2t + ((size_t)((ct * 54 + kk) * 5 + T5) * 64 + lane) * 8) = f;
      }
    }
  }
}

__global__ __launch_bounds__(256) void k1_conv(const u16* __restrict__ kvT, const u16* __restrict__ ssm_k,
                                               const u16* __restrict__ ssm_D, u16* __restrict__ y2T,
                                               const u16* __restrict__ c, const u16* __restrict__ w_mod, float* __restrict__ mod,
                                               const u16* __restrict__ w1, const u16* __restrict__ w2,
                                               u16* __restrict__ w1t, u16* __restrict__ w2t STAMP_ARG) {
  extern __shared__ __attribute__((aligned(16))) char lds_dyn[];
  LAS char* lds = (LAS char*)lds_dyn;
  STAMP_DECL
  STAMP(1, 0);
  const int b = blockIdx.x;
  if (b < K1_CONV_WG) conv_body(lds, kvT, ssm_k, ssm_D, y2T, b STAMP_FWD);
  else if (b < K1_CONV_WG + K1_MOD_WG) mod_body(lds, c, w_mod, mod, b - K1_CONV_WG);
  else tile_body(lds, w1, w2, w1t, w2t, b - K1_CONV_WG - K1_MOD_WG);
  STAMP(1, 1);
  STAMP_FLUSH(1);
}

constexpr int K2_CST_OFF = 81920;
constexpr int K2_LDS = K2_CST_OFF + 4 * 640 * 4;

struct RowStat { float rs, nm; };

__device__ __forceinline__ RowStat row_stats(const v4u (&f)[10], int lane) {
  const v4u ones = {0x3f803f80u, 0x3f803f80u, 0x3f803f80u, 0x3f803f80u};
  f32x4 d1 = {0.f, 0.f, 0.f, 0.f}, d2 = {0.f, 0.f, 0.f, 0.f};
#pragma unroll
  for (int m = 0; m < 10; ++m) { d1 = mfma16(ones, f[m], d1); d2 = mfma16(f[m], f[m], d2); }
  const int n = lane & 15;
  float dg = (n & 2) ? ((n & 1) ? d2[3] : d2[2]) : ((n & 1) ? d2[1] : d2[0]);
  const int src = (n + 16 * (n >> 2)) * 4;
  float sq = __int_as_float(__builtin_amdgcn_ds_bpermute(src, __float_as_int(dg)));
  float sm = d1[0];
  sm += __int_as_float(__builtin_amdgcn_update_dpp(0, __float_as_int(sm), 0x128, 0xf, 0xf, false));
  sq += __int_as_float(__builtin_amdgcn_update_dpp(0, __float_as_int(sq), 0x128, 0xf, 0xf, false));
  const float mean = sm * (1.f / D_);
  const float var = fmaxf(sq * (1.f / D_) - mean * mean, 0.f);
  const float rs = __builtin_amdgcn_rsqf(var + LN_EPS);
  return RowStat{rs, -mean * rs};
}

__device__ __forceinline__ float gelu_tanh(float x) {
  constexpr float C0 = -2.f * 0.7978845608028654f * 1.4426950408889634f;
  constexpr float C1 = C0 * 0.044715f;
  const float e = __builtin_amdgcn_exp2f(x * fmaf(x * x, C1, C0));
  return x * __builtin_amdgcn_rcpf(1.f + e);
}

#define GLD16(dst, ptr, off) asm volatile("global_load_dwordx4 %0, %1, off offset:" #off : "=v"(dst) : "v"(ptr) : "memory")
__device__ __forceinline__ void gld16(v4u& dst, const void* p) { asm volatile("global_load_dwordx4 %0, %1, off" : "=v"(dst) : "v"(p) : "memory"); }
template <int OFF> __device__ __forceinline__ void gld16o(v4u& dst, const void* p) {
  asm volatile("global_load_dwordx4 %0, %1, off offset:%c2" : "=v"(dst) : "v"(p), "i"(OFF) : "memory");
}
template <int N> __device__ __forceinline__ void wait_vm(v4u& a) { asm volatile("s_waitcnt vmcnt(%c1)" : "+v"(a) : "i"(N) : "memory"); }
template <int N> __device__ __forceinline__ void wait_vm(v4u& a, v4u& b) { asm volatile("s_waitcnt vmcnt(%c2)" : "+v"(a), "+v"(b) : "i"(N) : "memory"); }
__device__ __forceinline__ void tie(v4u& a) { asm volatile("" : "+v"(a)); }

template <int I, int N, class Fn> __device__ __forceinline__ void static_for(Fn&& f) {
  if constexpr (I < N) { f(std::integral_constant<int, I>{}); static_for<I + 1, N>(f); }
}

__global__ __launch_bounds__(512) void k2_ln_gemm1(const u16* __restrict__ x, const u16* __restrict__ y2T, const float* __restrict__ mod,
                                                   const u16* __restrict__ w1t, const u16* __restrict__ b1,
                                                   u16* __restrict__ x2g, u16* __restrict__ Ht STAMP_ARG) {
  extern __shared__ __attribute__((aligned(16))) char lds_dyn[];
  LAS char* lds = (LAS char*)lds_dyn;
  STAMP_DECL
  STAMP(2, 0);
  const int tid = threadIdx.x, lane = tid & 63, wave = __builtin_amdgcn_readfirstlane(tid >> 6);
  const int b = blockIdx.x, xl = b & 7, mI = b >> 3;
  const int rt = xl + 8 * (mI >> 3), cg8 = mI & 7;
  const int P0 = cg8 < 6 ? 7 * cg8 : 42 + 6 * (cg8 - 6);
  const int np = cg8 < 6 ? 7 : 6;
  const int r = lane & 7, cg = lane >> 3;
  const int row = 64 * rt + 8 * wave + r;
  v4u xw[10];
  {
    const u16* xp = x + (size_t)row * D_ + 8 * cg;
    static_for<0, 10>([&](auto m) { gld16o<128 * m>(xw[m], xp); });
  }
  v4u md[5];
  {
    const float* mp = mod + 4 * (tid < 160 ? tid : 159);
#pragma unroll
    for (int j = 0; j < 5; ++j) gld16(md[j], mp + j * D_);
  }
  {
    const int chl = lane >> 3, pos = lane & 7;
    const unsigned l0 = lds_addr_of(lds) + wave * 10240;
#pragma unroll
    for (int i = 0; i < 10; ++i) {
      const int I = wave * 10 + i, ch = 8 * I + chl;
      const int f = ((ch >> 1) & 1) | (((ch >> 3) & 1) << 1) | (((ch >> 4) & 1) << 2);
      glds16_asm(y2T + (size_t)ch * S_ + 64 * rt + 8 * (pos ^ f), l0 + i * 1024);
    }
  }
  asm volatile("s_waitcnt vmcnt(10)"
               : "+v"(xw[0]), "+v"(xw[1]), "+v"(xw[2]), "+v"(xw[3]), "+v"(xw[4]), "+v"(xw[5]), "+v"(xw[6]), "+v"(xw[7]), "+v"(xw[8]), "+v"(xw[9]),
                 "+v"(md[0]), "+v"(md[1]), "+v"(md[2]), "+v"(md[3]), "+v"(md[4]) :: "memory");
  LAS float* cst = (LAS float*)(lds + K2_CST_OFF);
  if (tid < 160) {
    const f32x4 sa = __builtin_bit_cast(f32x4, md[0]), ca = __builtin_bit_cast(f32x4, md[1]), ga = __builtin_bit_cast(f32x4, md[2]);
    const f32x4 sb = __builtin_bit_cast(f32x4, md[3]), cb = __builtin_bit_cast(f32x4, md[4]);
    f32x4 g1, a2;
#pragma unroll
    for (int j = 0; j < 4; ++j) { g1[j] = ga[j] * rnd_bf16(1.f + ca[j]); a2[j] = rnd_bf16(1.f + cb[j]); }
    *(LAS f32x4*)(cst + 4 * tid) = g1;
    *(LAS f32x4*)(cst + D_ + 4 * tid) = ga * sa;
    *(LAS f32x4*)(cst + 2 * D_ + 4 * tid) = a2;
    *(LAS f32x4*)(cst + 3 * D_ + 4 * tid) = sb;
  }
  const RowStat s1 = row_stats(xw, lane);
  WAIT_LGKM0();
  BARRIER();
  CSTAMP(2, 5);
  f32x2 val[10][4];
  LAS float* cl = cst + 8 * cg;
  {
    f32x4 c0[2][2], c1[2][2];
    c0[0][0] = *(LAS f32x4*)(cl); c0[0][1] = *(LAS f32x4*)(cl + 4);
    c1[0][0] = *(LAS f32x4*)(cl + D_); c1[0][1] = *(LAS f32x4*)(cl + D_ + 4);
    const f32x2 rs2 = {s1.rs, s1.rs}, nm2 = {s1.nm, s1.nm};
#pragma unroll
    for (int m = 0; m < 10; ++m) {
      const int cu = m & 1, nx = cu ^ 1;
      if (m < 9) {
        c0[nx][0] = *(LAS f32x4*)(cl + 64 * (m + 1)); c0[nx][1] = *(LAS f32x4*)(cl + 64 * (m + 1) + 4);
        c1[nx][0] = *(LAS f32x4*)(cl + D_ + 64 * (m + 1)); c1[nx][1] = *(LAS f32x4*)(cl + D_ + 64 * (m + 1) + 4);
      }
#pragma unroll
      for (int j = 0; j < 4; ++j) {
        const f32x2 xf = {bf_lo(xw[m][j]), bf_hi(xw[m][j])};
        const f32x2 G1 = {c0[cu][j >> 1][2 * (j & 1)], c0[cu][j >> 1][2 * (j & 1) + 1]};
        const f32x2 G2 = {c1[cu][j >> 1][2 * (j & 1)], c1[cu][j >> 1][2 * (j & 1) + 1]};
        val[m][j] = __builtin_elementwise_fma(__builtin_elementwise_fma(xf, rs2, nm2), G1, G2);
      }
      SCHED_FENCE();
    }
  }
  WAIT_VM(0);
  BARRIER();
  CSTAMP(2, 2);
  constexpr int PF = 5;
  const int P = P0 + (wave < np ? wave : np - 1);
  const u16* wp = w1t + ((size_t)P * 40 * 64 + lane) * 8;
  v4u wa[20][2];
  static_for<0, PF>([&](auto kk) { gld16(wa[kk][0], wp + (kk * 2) * 512); gld16(wa[kk][1], wp + (kk * 2 + 1) * 512); });
  v4u pk[10];
  {
    const int G = lane >> 4, q = (lane & 15) >> 2, p = lane & 3;
    const int chb = 8 * (2 * G + (p >> 1)) + q;
    const int fb = ((chb >> 1) & 1) | (((chb >> 3) & 1) << 1) | (((chb >> 4) & 1) << 2);
    const int fc = (((chb + 4) >> 1) & 1) | ((((chb + 4) >> 3) & 1) << 1) | ((((chb + 4) >> 4) & 1) << 2);
    LAS char* a0 = lds + chb * 128 + ((wave ^ fb) << 4) + 8 * (p & 1);
    LAS char* a1 = lds + (chb + 4) * 128 + ((wave ^ fc) << 4) + 8 * (p & 1);
    v4u yw[10];
    yw[0] = tr_read2(a0, a1);
    yw[1] = tr_read2(a0 + 8192, a1 + 8192);
#pragma unroll
    for (int m = 0; m < 10; ++m) {
      if (m + 2 < 10) yw[m + 2] = tr_read2(a0 + (m + 2) * 8192, a1 + (m + 2) * 8192);
#pragma unroll
      for (int j = 0; j < 4; ++j) {
        const f32x2 xf = {bf_lo(xw[m][j]), bf_hi(xw[m][j])};
        const f32x2 yf = {bf_lo(yw[m][j]), bf_hi(yw[m][j])};
        val[m][j] = __builtin_elementwise_fma(yf, val[m][j], xf);
        pk[m][j] = pk_bf16(val[m][j][0], val[m][j][1]);
      }
      SCHED_FENCE();
    }
  }
  if (cg8 >= 6) {
    u16* xp = x2g + (size_t)row * D_ + 8 * cg;
    if (cg8 == 6) {
#pragma unroll
      for (int m = 0; m < 5; ++m) *(v4u*)(xp + 64 * m) = pk[m];
    } else {
#pragma unroll
      for (int m = 5; m < 10; ++m) *(v4u*)(xp + 64 * m) = pk[m];
    }
  }
  WAIT_LGKM0();
  BARRIER();
  CSTAMP(2, 6);
  {
    f32x4 c0[2][2], c1[2][2];
    c0[0][0] = *(LAS f32x4*)(cl + 2 * D_); c0[0][1] = *(LAS f32x4*)(cl + 2 * D_ + 4);
    c1[0][0] = *(LAS f32x4*)(cl + 3 * D_); c1[0][1] = *(LAS f32x4*)(cl + 3 * D_ + 4);
    const RowStat s2 = row_stats(pk, lane);
    const f32x2 rs2 = {s2.rs, s2.rs}, nm2 = {s2.nm, s2.nm};
#pragma unroll
    for (int m = 0; m < 10; ++m) {
      const int cu = m & 1, nx = cu ^ 1;
      if (m < 9) {
        c0[nx][0] = *(LAS f32x4*)(cl + 2 * D_ + 64 * (m + 1)); c0[nx][1] = *(LAS f32x4*)(cl + 2 * D_ + 64 * (m + 1) + 4);
        c1[nx][0] = *(LAS f32x4*)(cl + 3 * D_ + 64 * (m + 1)); c1[nx][1] = *(LAS f32x4*)(cl + 3 * D_ + 64 * (m + 1) + 4);
      }
#pragma unroll
      for (int j = 0; j < 4; ++j) {
        const f32x2 A2 = {c0[cu][j >> 1][2 * (j & 1)], c0[cu][j >> 1][2 * (j & 1) + 1]};
        const f32x2 S2 = {c1[cu][j >> 1][2 * (j & 1)], c1[cu][j >> 1][2 * (j & 1) + 1]};
        val[m][j] = __builtin_elementwise_fma(__builtin_elementwise_fma(val[m][j], rs2, nm2), A2, S2);
        pk[m][j] = pk_bf16(val[m][j][0], val[m][j][1]);
      }
      SCHED_FENCE();
    }
  }
  {
    const RowStat s3 = row_stats(pk, lane);
    const f32x2 rs2 = {s3.rs, s3.rs}, nm2 = {s3.nm, s3.nm};
    const int mt = wave >> 1, n = 8 * (wave & 1) + r, g2 = cg & 3;
    LAS char* yb = lds + ((mt * 20 + (cg >> 2)) * 64 + n + 16 * g2) * 16;
#pragma unroll
    for (int m = 0; m < 10; ++m) {
      v4u o;
#pragma unroll
      for (int j = 0; j < 4; ++j) { const f32x2 t = __builtin_elementwise_fma(val[m][j], rs2, nm2); o[j] = pk_bf16(t[0], t[1]); }
      *(LAS v4u*)(yb + m * 2048) = o;
    }
  }
  WAIT_LGKM0();
  BARRIER();
  CSTAMP(2, 3);
  if (wave < np) {
    f32x4 acc[4][2];
#pragma unroll
    for (int mt = 0; mt < 4; ++mt) { acc[mt][0] = f32x4{0.f, 0.f, 0.f, 0.f}; acc[mt][1] = f32x4{0.f, 0.f, 0.f, 0.f}; }
    const v4u bias = *(const v4u*)(b1 + 32 * P + 8 * (lane >> 4));
    LAS char* bl = lds + lane * 16;
    v4u bf[2][4];
#pragma unroll
    for (int mt = 0; mt < 4; ++mt) bf[0][mt] = *(LAS v4u*)(bl + (mt * 20) * 1024);
    static_for<0, 20>([&](auto kc) {
      constexpr int kk = kc, cu = kk & 1, nx = cu ^ 1;
      if constexpr (kk + PF < 20) { gld16(wa[kk + PF][0], wp + ((kk + PF) * 2) * 512); gld16(wa[kk + PF][1], wp + ((kk + PF) * 2 + 1) * 512); }
      constexpr int younger = 2 * ((kk + PF < 20 ? kk + PF : 19) - kk);
      wait_vm<younger>(wa[kk][0], wa[kk][1]);
      if constexpr (kk < 19) {
#pragma unroll
        for (int mt = 0; mt < 4; ++mt) bf[nx][mt] = *(LAS v4u*)(bl + (mt * 20 + kk + 1) * 1024);
      }
#pragma unroll
      for (int mt = 0; mt < 4; ++mt) {
        acc[mt][0] = mfma16(wa[kk][0], bf[cu][mt], acc[mt][0]);
        acc[mt][1] = mfma16(wa[kk][1], bf[cu][mt], acc[mt][1]);
      }
      if constexpr (kk < 19) {
#pragma unroll
        for (int i = 0; i < 4; ++i) { SGB(SG_MFMA, 1); SGB(SG_DS_R, 1); }
        SGB(SG_MFMA, 4);
      }
      SCHED_FENCE();
    });
    CSTAMP(2, 4);
    const float bb[8] = {bf_lo(bias.x), bf_hi(bias.x), bf_lo(bias.y), bf_hi(bias.y), bf_lo(bias.z), bf_hi(bias.z), bf_lo(bias.w), bf_hi(bias.w)};
#pragma unroll
    for (int mt = 0; mt < 4; ++mt) {
      float h[8];
#pragma unroll
      for (int j = 0; j < 8; ++j) h[j] = gelu_tanh(acc[mt][j >> 2][j & 3] + bb[j]);
      *(v4u*)(Ht + (((size_t)(4 * rt + mt) * 54 + P) * 64 + lane) * 8) =
          v4u{pk_bf16(h[0], h[1]), pk_bf16(h[2], h[3]), pk_bf16(h[4], h[5]), pk_bf16(h[6], h[7])};
    }
  } else {
    WAIT_VM(0);
  }
  STAMP(2, 1);
  STAMP_FLUSH(2);
}

constexpr int K3_STAGE = 6 * 5 * 1024;
constexpr int K3_NS = 4;
constexpr int K3_LDS = K3_NS * K3_STAGE;

__device__ __forceinline__ void glds16x4(unsigned voff, const void* sbase, unsigned lds_addr) {
  asm volatile("s_mov_b32 m0, %2\n\ts_nop 0\n\t"
               "global_load_lds_dwordx4 %0, %1\n\tglobal_load_lds_dwordx4 %0, %1 offset:1024\n\t"
               "global_load_lds_dwordx4 %0, %1 offset:2048\n\tglobal_load_lds_dwordx4 %0, %1 offset:3072"
               :: "v"(voff), "s"(sbase), "s"(lds_addr) : "memory");
}
__device__ __forceinline__ void glds16x2(unsigned voff, const void* sbase, unsigned lds_addr) {
  asm volatile("s_mov_b32 m0, %2\n\ts_nop 0\n\t"
               "global_load_lds_dwordx4 %0, %1\n\tglobal_load_lds_dwordx4 %0, %1 offset:1024"
               :: "v"(voff), "s"(sbase), "s"(lds_addr) : "memory");
}

__global__ __launch_bounds__(320) void k3_gemm2(const u16* __restrict__ Ht, const u16* __restrict__ w2t, const u16* __restrict__ x2g,
                                                const float* __restrict__ mod, const u16* __restrict__ b2, u16* __restrict__ out STAMP_ARG) {
  extern __shared__ __attribute__((aligned(16))) char lds_dyn[];
  LAS char* lds = (LAS char*)lds_dyn;
  STAMP_DECL
  STAMP(3, 0);
  const int tid = threadIdx.x, lane = tid & 63, wave = __builtin_amdgcn_readfirstlane(tid >> 6);
  const int b = blockIdx.x, xl = b & 7, mI = b >> 3;
  const int rt = xl + 8 * (mI >> 3), ct = mI & 7;
  if (wave == 4) {
    const char* src = (const char*)w2t + (size_t)ct * 54 * 5 * 1024;
    const unsigned voff = lane * 16, l0 = lds_addr_of(lds);
    auto issue = [&](int s) {
      const char* sp = src + s * K3_STAGE;
      const unsigned dp = l0 + (s % K3_NS) * K3_STAGE;
#pragma unroll
      for (int i = 0; i < 7; ++i) glds16x4(voff, sp + i * 4096, dp + i * 4096);
      glds16x2(voff, sp + 7 * 4096, dp + 7 * 4096);
    };
    issue(0); issue(1);
#pragma unroll 1
    for (int s = 0; s < 7; ++s) {
      WAIT_VM(30);
      BARRIER();
      issue(s + 2);
    }
    WAIT_VM(30);
    BARRIER();
    WAIT_VM(0);
    BARRIER();
  } else {
    const u16* hp = Ht + ((size_t)(4 * rt + wave) * 54 * 64 + lane) * 8;
    constexpr int PF = 12;
    v4u hb[54];
    static_for<0, PF>([&](auto kk) { gld16(hb[kk], hp + kk * 512); });
    const int n = lane & 15, g = lane >> 4;
    const int row = 64 * rt + 16 * wave + n;
    const int f0 = 80 * ct + 8 * g, f2 = 80 * ct + 64 + 4 * g;
    f32x4 acc[5];
#pragma unroll
    for (int t = 0; t < 5; ++t) acc[t] = f32x4{0.f, 0.f, 0.f, 0.f};
    LAS char* lb = lds + lane * 16;
    v4u wa[2][5];
    BARRIER();
#pragma unroll
    for (int t = 0; t < 5; ++t) wa[0][t] = *(LAS v4u*)(lb + t * 1024);
    static_for<0, 54>([&](auto kc) {
      constexpr int kk = kc, cu = kk & 1, nx = cu ^ 1;
      if constexpr (kk + PF < 54) gld16(hb[kk + PF], hp + (kk + PF) * 512);
      constexpr int younger = (kk + PF < 54 ? kk + PF : 53) - kk;
      wait_vm<younger>(hb[kk]);
      if constexpr (kk % 6 == 5 && kk < 53) { WAIT_LGKM0(); BARRIER(); }
      if constexpr (kk < 53) {
        constexpr int s1 = (kk + 1) / 6, j1 = (kk + 1) % 6;
#pragma unroll
        for (int t = 0; t < 5; ++t) wa[nx][t] = *(LAS v4u*)(lb + (s1 % K3_NS) * K3_STAGE + (j1 * 5 + t) * 1024);
      }
#pragma unroll
      for (int t = 0; t < 5; ++t) acc[t] = mfma16(wa[cu][t], hb[kk], acc[t]);
      if constexpr (kk < 53) {
#pragma unroll
        for (int i = 0; i < 5; ++i) { SGB(SG_MFMA, 1); SGB(SG_DS_R, 1); }
      }
      SCHED_FENCE();
    });
    CSTAMP(3, 2);
    const v4u xa = *(const v4u*)(x2g + (size_t)row * D_ + f0), xb = *(const v4u*)(x2g + (size_t)row * D_ + f0 + 32);
    const v2u xc = *(const v2u*)(x2g + (size_t)row * D_ + f2);
    const float* gb = mod + 5 * D_;
    const v4u ba = *(const v4u*)(b2 + f0), bbv = *(const v4u*)(b2 + f0 + 32);
    const v2u bc = *(const v2u*)(b2 + f2);
    const f32x4 ga0 = *(const f32x4*)(gb + f0), ga1 = *(const f32x4*)(gb + f0 + 4);
    const f32x4 gb0 = *(const f32x4*)(gb + f0 + 32), gb1 = *(const f32x4*)(gb + f0 + 36);
    const f32x4 gc0 = *(const f32x4*)(gb + f2);
    float o[8];
#pragma unroll
    for (int j = 0; j < 8; ++j) {
      const float xv = (j & 1) ? bf_hi(xa[j >> 1]) : bf_lo(xa[j >> 1]);
      const float bv = (j & 1) ? bf_hi(ba[j >> 1]) : bf_lo(ba[j >> 1]);
      o[j] = fmaf(j < 4 ? ga0[j & 3] : ga1[j & 3], acc[j >> 2][j & 3] + bv, xv);
    }
    *(v4u*)(out + (size_t)row * D_ + f0) = v4u{pk_bf16(o[0], o[1]), pk_bf16(o[2], o[3]), pk_bf16(o[4], o[5]), pk_bf16(o[6], o[7])};
#pragma unroll
    for (int j = 0; j < 8; ++j) {
      const float xv = (j & 1) ? bf_hi(xb[j >> 1]) : bf_lo(xb[j >> 1]);
      const float bv = (j & 1) ? bf_hi(bbv[j >> 1]) : bf_lo(bbv[j >> 1]);
      o[j] = fmaf(j < 4 ? gb0[j & 3] : gb1[j & 3], acc[2 + (j >> 2)][j & 3] + bv, xv);
    }
    *(v4u*)(out + (size_t)row * D_ + f0 + 32) = v4u{pk_bf16(o[0], o[1]), pk_bf16(o[2], o[3]), pk_bf16(o[4], o[5]), pk_bf16(o[6], o[7])};
#pragma unroll
    for (int j = 0; j < 4; ++j) {
      const float xv = (j & 1) ? bf_hi(xc[j >> 1]) : bf_lo(xc[j >> 1]);
      const float bv = (j & 1) ? bf_hi(bc[j >> 1]) : bf_lo(bc[j >> 1]);
      o[j] = fmaf(gc0[j], acc[4][j] + bv, xv);
    }
    *(v2u*)(out + (size_t)row * D_ + f2) = v2u{pk_bf16(o[0], o[1]), pk_bf16(o[2], o[3])};
  }
  STAMP(3, 1);
  STAMP_FLUSH(3);
}

template <int N> __global__ void code_k(float* p, int n) {
  float a = threadIdx.x, b = a + 1, c = a + 2, d = a + 3, e = 1.0001f;
#pragma unroll
  for (int i = 0; i < N; i++) asm volatile("v_fma_f32 %0, %0, %4, %4\n\tv_fma_f32 %1, %1, %4, %4\n\tv_fma_f32 %2, %2, %4, %4\n\tv_fma_f32 %3, %3, %4, %4\n\tv_fma_f32 %0, %0, %4, %4\n\tv_fma_f32 %1, %1, %4, %4\n\tv_fma_f32 %2, %2, %4, %4\n\tv_fma_f32 %3, %3, %4, %4" : "+v"(a),"+v"(b),"+v"(c),"+v"(d) : "v"(e));
  if (n == 12345) p[threadIdx.x] = a + b + c + d;
}
template <int N> __global__ void loop_k(float* p, int n) {
  float a = threadIdx.x, b = a + 1, c = a + 2, d = a + 3, e = 1.0001f;
#pragma unroll 1
  for (int i = 0; i < N; i++) asm volatile("v_fma_f32 %0, %0, %4, %4\n\tv_fma_f32 %1, %1, %4, %4\n\tv_fma_f32 %2, %2, %4, %4\n\tv_fma_f32 %3, %3, %4, %4\n\tv_fma_f32 %0, %0, %4, %4\n\tv_fma_f32 %1, %1, %4, %4\n\tv_fma_f32 %2, %2, %4, %4\n\tv_fma_f32 %3, %3, %4, %4" : "+v"(a),"+v"(b),"+v"(c),"+v"(d) : "v"(e));
  if (n == 12345) p[threadIdx.x] = a + b + c + d;
}
extern "C" void kernel_launch(void* const* d_in, const int* in_sizes, int n_in, void* d_out, int out_size,
                              void* d_ws, size_t ws_size, hipStream_t stream) {
  const u16* q = (const u16*)d_in[0];
  const u16* k = (const u16*)d_in[1];
  const u16* v = (const u16*)d_in[2];
  const u16* shift_k = (const u16*)d_in[3];
  const u16* ssm_k = (const u16*)d_in[4];
  const u16* ssm_D = (const u16*)d_in[5];
  const u16* w1 = (const u16*)d_in[7];
  const u16* b1 = (const u16*)d_in[8];
  const u16* w2 = (const u16*)d_in[9];
  const u16* b2 = (const u16*)d_in[10];
  const u16* c = (const u16*)d_in[11];
  const u16* w_mod = (const u16*)d_in[12];
  char* ws = (char*)d_ws;
  u16* kvT = (u16*)(ws + WS_KVT);
  u16* y2T = (u16*)(ws + WS_Y2T);
  u16* x2 = (u16*)(ws + WS_X2);
  u16* Ht = (u16*)(ws + WS_HT);
  u16* w1t = (u16*)(ws + WS_W1T);
  u16* w2t = (u16*)(ws + WS_W2T);
  float* mod = (float*)(ws + WS_MOD);
  (void)hipFuncSetAttribute((const void*)k1_conv, hipFuncAttributeMaxDynamicSharedMemorySize, K1_LDS);
  (void)hipFuncSetAttribute((const void*)k2_ln_gemm1, hipFuncAttributeMaxDynamicSharedMemorySize, K2_LDS);
  (void)hipFuncSetAttribute((const void*)k3_gemm2, hipFuncAttributeMaxDynamicSharedMemorySize, K3_LDS);
  k0_kv<<<320, 256, 0, stream>>>(k, v, shift_k, kvT STAMP_PASS);
  k1_conv<<<256, 256, K1_LDS, stream>>>(kvT, ssm_k, ssm_D, y2T, c, w_mod, mod, w1, w2, w1t, w2t STAMP_PASS);
  code_k<1><<<256, 64, 0, stream>>>((float*)(ws + WS_STAMP), 0);
  k2_ln_gemm1<<<256, 512, K2_LDS, stream>>>(q, y2T, mod, w1t, b1, x2, Ht STAMP_PASS);
  k3_gemm2<<<256, 320, K3_LDS, stream>>>(Ht, w2t, x2, mod, b2, (u16*)d_out STAMP_PASS);
}
```

```cpp
#include <hip/hip_runtime.h>
#include <hip/hip_bf16.h>
#include <type_traits>

#define TILE_NT 1
#define CONV_UNROLL 4
#define LAS __attribute__((address_space(3)))
#define GAS __attribute__((address_space(1)))
typedef unsigned short u16;
typedef unsigned v4u __attribute__((ext_vector_type(4)));
typedef unsigned v2u __attribute__((ext_vector_type(2)));
typedef short s16x4 __attribute__((ext_vector_type(4)));
typedef __bf16 bf16x8 __attribute__((ext_vector_type(8)));
typedef __bf16 bf16x2 __attribute__((ext_vector_type(2)));
typedef float f32x4 __attribute__((ext_vector_type(4)));
typedef float f32x2 __attribute__((ext_vector_type(2)));

constexpr int S_ = 2048, D_ = 640, F_ = 1728;
constexpr int SP_ = S_ + 64;
constexpr float LN_EPS = 1e-5f;

constexpr size_t WS_KVT = 0;
constexpr size_t WS_Y2T = WS_KVT + (size_t)D_ * SP_ * 2;
constexpr size_t WS_X2  = WS_Y2T + (size_t)D_ * SP_ * 2;
constexpr size_t WS_HT  = WS_X2 + (size_t)D_ * S_ * 2;
constexpr size_t WS_W1T = WS_HT + (size_t)S_ * F_ * 2;
constexpr size_t WS_W2T = WS_W1T + (size_t)D_ * F_ * 2;
constexpr size_t WS_MOD = WS_W2T + (size_t)128 * F_ * 2;
constexpr size_t WS_TAG = WS_MOD + 4 * 3840 * 4;
constexpr size_t WS_F0 = WS_TAG + 128;
constexpr size_t WS_FM = WS_F0 + 320 * 128;
constexpr size_t WS_F1 = WS_FM + 240 * 128;
constexpr size_t WS_FT = WS_F1 + 160 * 128;
constexpr size_t WS_F2 = WS_FT + 256 * 128;
constexpr size_t WS_STAMP = WS_F2 + 256 * 128;

#define STAMP_DECL
#define STAMP(kid, slot) do {} while (0)
#define CSTAMP(kid, slot) do {} while (0)
#define STAMP_FLUSH(kid) do {} while (0)
#define STAMP_ARG
#define STAMP_PASS
#define STAMP_FWD
#define STAMP_PARAM

__device__ __forceinline__ float bf_lo(unsigned w) { return __uint_as_float(w << 16); }
__device__ __forceinline__ float bf_hi(unsigned w) { return __uint_as_float(w & 0xffff0000u); }
__device__ __forceinline__ unsigned pk_bf16(float lo, float hi) {
  f32x2 p = {lo, hi};
  return __builtin_bit_cast(unsigned, __builtin_convertvector(p, bf16x2));
}
__device__ __forceinline__ float rnd_bf16(float x) { return bf_lo(pk_bf16(x, 0.f)); }
__device__ __forceinline__ bf16x8 as_frag(v4u v) { return __builtin_bit_cast(bf16x8, v); }
__device__ __forceinline__ f32x4 mfma16(v4u a, v4u b, f32x4 c) {
  return __builtin_amdgcn_mfma_f32_16x16x32_bf16(as_frag(a), as_frag(b), c, 0, 0, 0);
}
__device__ __forceinline__ v2u tr_read(LAS char* p) {
  s16x4 r = __builtin_amdgcn_ds_read_tr16_b64_v4i16((LAS s16x4*)p);
  return __builtin_bit_cast(v2u, r);
}
__device__ __forceinline__ v4u tr_read2(LAS char* p0, LAS char* p1) {
  v2u a = tr_read(p0), b = tr_read(p1);
  return v4u{a.x, a.y, b.x, b.y};
}
__device__ __forceinline__ v4u ld_nt16(const void* p) { return __builtin_nontemporal_load((const v4u*)p); }
#define GLDS16(src, dst) __builtin_amdgcn_global_load_lds((const GAS unsigned*)(src), (LAS unsigned*)(dst), 16, 0, 0)
#define GLDS16_NT(src, dst) __builtin_amdgcn_global_load_lds((const GAS unsigned*)(src), (LAS unsigned*)(dst), 16, 0, 2)
#define WAIT_VM(n) asm volatile("s_waitcnt vmcnt(" #n ")" ::: "memory")
#define WAIT_LGKM0() asm volatile("s_waitcnt lgkmcnt(0)" ::: "memory")
#define BARRIER() __builtin_amdgcn_s_barrier()
#define SCHED_FENCE() __builtin_amdgcn_sched_barrier(0)
#define SGB(mask, n) __builtin_amdgcn_sched_group_barrier(mask, n, 0)
constexpr int SG_MFMA = 0x008, SG_VMEM_R = 0x020, SG_DS_R = 0x100;
__device__ __forceinline__ void glds16_asm(const void* gsrc, unsigned lds_addr) {
  asm volatile("s_mov_b32 m0, %1\n\ts_nop 0\n\tglobal_load_lds_dwordx4 %0, off" :: "v"(gsrc), "s"(lds_addr) : "memory");
}
__device__ __forceinline__ int fresh_lane() { int l = threadIdx.x & 63; asm volatile("" : "+v"(l)); return l; }
__device__ __forceinline__ unsigned lds_addr_of(LAS char* p) { return (unsigned)(size_t)p; }

#define GLD16(dst, ptr, off) asm volatile("global_load_dwordx4 %0, %1, off offset:" #off : "=v"(dst) : "v"(ptr) : "memory")
__device__ __forceinline__ void gld16(v4u& dst, const void* p) { asm volatile("global_load_dwordx4 %0, %1, off" : "=v"(dst) : "v"(p) : "memory"); }
template <int OFF> __device__ __forceinline__ void gld16o(v4u& dst, const void* p) {
  asm volatile("global_load_dwordx4 %0, %1, off offset:%c2" : "=v"(dst) : "v"(p), "i"(OFF) : "memory");
}
template <int OFF> __device__ __forceinline__ void gld16s(v4u& dst, unsigned voff, const void* sbase) {
  asm volatile("global_load_dwordx4 %0, %1, %2 offset:%c3" : "=v"(dst) : "v"(voff), "s"(sbase), "i"(OFF) : "memory");
}
template <int N> __device__ __forceinline__ void wait_vm(v4u& a) { asm volatile("s_waitcnt vmcnt(%c1)" : "+v"(a) : "i"(N) : "memory"); }
template <int N> __device__ __forceinline__ void wait_vm(v4u& a, v4u& b) { asm volatile("s_waitcnt vmcnt(%c2)" : "+v"(a), "+v"(b) : "i"(N) : "memory"); }
template <int N> __device__ __forceinline__ void wait_vm_only() { asm volatile("s_waitcnt vmcnt(%c0)" :: "i"(N) : "memory"); }
__device__ __forceinline__ void tie(v4u& a) { asm volatile("" : "+v"(a)); }

template <int I, int N, class Fn> __device__ __forceinline__ void static_for(Fn&& f) {
  if constexpr (I < N) { f(std::integral_constant<int, I>{}); static_for<I + 1, N>(f); }
}

__device__ __forceinline__ void glds16_s(unsigned voff, const void* sbase, unsigned lds_addr) {
  asm volatile("s_mov_b32 m0, %2\n\ts_nop 0\n\tglobal_load_lds_dwordx4 %0, %1" :: "v"(voff), "s"(sbase), "s"(lds_addr) : "memory");
}
__device__ __forceinline__ void glds16_s_sc1(unsigned voff, const void* sbase, unsigned lds_addr) {
  asm volatile("s_mov_b32 m0, %2\n\ts_nop 0\n\tglobal_load_lds_dwordx4 %0, %1 sc1" :: "v"(voff), "s"(sbase), "s"(lds_addr) : "memory");
}
__device__ __forceinline__ void gld16_sc1(v4u& dst, const void* p) { asm volatile("global_load_dwordx4 %0, %1, off sc1" : "=v"(dst) : "v"(p) : "memory"); }
__device__ __forceinline__ void glds4_s(unsigned voff, const void* sbase, unsigned lds_addr) {
  asm volatile("s_mov_b32 m0, %2\n\ts_nop 0\n\tglobal_load_lds_dword %0, %1" :: "v"(voff), "s"(sbase), "s"(lds_addr) : "memory");
}

__device__ __forceinline__ void glds16x4(unsigned voff, const void* sbase, unsigned lds_addr) {
  asm volatile("s_mov_b32 m0, %2\n\ts_nop 0\n\t"
               "global_load_lds_dwordx4 %0, %1 sc1\n\tglobal_load_lds_dwordx4 %0, %1 offset:1024 sc1\n\t"
               "global_load_lds_dwordx4 %0, %1 offset:2048 sc1\n\tglobal_load_lds_dwordx4 %0, %1 offset:3072 sc1"
               :: "v"(voff), "s"(sbase), "s"(lds_addr) : "memory");
}
__device__ __forceinline__ void glds16x2(unsigned voff, const void* sbase, unsigned lds_addr) {
  asm volatile("s_mov_b32 m0, %2\n\ts_nop 0\n\t"
               "global_load_lds_dwordx4 %0, %1 sc1\n\tglobal_load_lds_dwordx4 %0, %1 offset:1024 sc1"
               :: "v"(voff), "s"(sbase), "s"(lds_addr) : "memory");
}

template <int OFF> __device__ __forceinline__ void gld16s_sc1(v4u& dst, unsigned voff, const void* sbase) {
  asm volatile("global_load_dwordx4 %0, %1, %2 offset:%c3 sc1" : "=v"(dst) : "v"(voff), "s"(sbase), "i"(OFF) : "memory");
}
typedef GAS unsigned gu32;
constexpr unsigned SPIN_CAP = 1u << 16;
#define TILE_DELAY 3
#define UPPER_WAIT 0
#define MOD_WAIT 0
#define G1_PRIO 0
#define G1_WIN 11
#define G2_DSN 4
#define G2_DSK 2
#define DUMP_PACE 1
#define YW0 0
#define YG 5
#define G1_BD 2
#define W2_PACE 0
#define SL0 1
#define SLM 4
#define SL1 1
#define SL2 1

__device__ __forceinline__ float dpp_ror8(float v) { return __int_as_float(__builtin_amdgcn_update_dpp(0, __float_as_int(v), 0x128, 0xf, 0xf, false)); }
__device__ __forceinline__ unsigned lds_arrive(LAS char* lds_sync, int word, int lane) {
  unsigned old = 0;
  if (lane == 0) old = __hip_atomic_fetch_add((LAS unsigned*)lds_sync + word, 1u, __ATOMIC_RELAXED, __HIP_MEMORY_SCOPE_WORKGROUP);
  return __builtin_amdgcn_readfirstlane(old);
}
__device__ __forceinline__ void lds_signal(LAS char* lds_sync, int word, int lane) {
  if (lane == 0) (void)__hip_atomic_fetch_add((LAS unsigned*)lds_sync + word, 1u, __ATOMIC_RELAXED, __HIP_MEMORY_SCOPE_WORKGROUP);
}
__device__ __forceinline__ unsigned lds_peek(LAS char* lds_sync, int word) {
  return __hip_atomic_load((LAS unsigned*)lds_sync + word, __ATOMIC_RELAXED, __HIP_MEMORY_SCOPE_WORKGROUP);
}
__device__ __forceinline__ void lds_wait4(LAS char* lds_sync, int word) {
#pragma clang loop unroll(disable)
  for (unsigned spins = 0; spins < (1u << 20); ++spins) {
    asm volatile("" ::: "memory");
    const v4u v = *(volatile LAS v4u*)((LAS unsigned*)lds_sync + word);
    if (__builtin_amdgcn_readfirstlane(v.x & v.y & v.z & v.w) != 0) break;
    __builtin_amdgcn_s_sleep(1);
  }
  asm volatile("" ::: "memory");
}
__device__ __forceinline__ void lds_wait8(LAS char* lds_sync, int word, int n) {
  const int lane = threadIdx.x & 63;
#pragma clang loop unroll(disable)
  for (unsigned spins = 0; spins < (1u << 20); ++spins) {
    const unsigned v = *(volatile LAS unsigned*)((LAS unsigned*)lds_sync + word + (lane < n ? lane : 0));
    if (__all(v != 0)) break;
    __builtin_amdgcn_s_sleep(1);
  }
  asm volatile("" ::: "memory");
}
__device__ __forceinline__ void lds_wait(LAS char* lds_sync, int word, unsigned count) {
#pragma clang loop unroll(disable)
  for (unsigned spins = 0; spins < (1u << 20); ++spins) {
    const unsigned v = __hip_atomic_load((LAS unsigned*)lds_sync + word, __ATOMIC_RELAXED, __HIP_MEMORY_SCOPE_WORKGROUP);
    if (__builtin_amdgcn_readfirstlane(v) >= count) break;
    __builtin_amdgcn_s_sleep(1);
  }
  asm volatile("" ::: "memory");
}

__device__ __forceinline__ __amdgpu_buffer_rsrc_t rsrc_of(const void* p, unsigned bytes) {
  return __builtin_amdgcn_make_buffer_rsrc((void*)p, 0, bytes, 0x00020000);
}
__device__ __forceinline__ void flag_store(char* base, int idx, unsigned tag) {
  __hip_atomic_store((gu32*)(base) + 32 * idx, tag, __ATOMIC_RELAXED, __HIP_MEMORY_SCOPE_AGENT);
}
template <int N, int SLEEP> __device__ __forceinline__ void poll_flags(const char* base, unsigned tag, int lane) {
  constexpr int R = (N + 63) / 64;
  bool seen[R];
#pragma unroll
  for (int i = 0; i < R; ++i) seen[i] = lane + 64 * i >= N;
#pragma clang loop unroll(disable)
  for (unsigned spins = 0; spins < SPIN_CAP; ++spins) {
    unsigned v[R];
#pragma unroll
    for (int i = 0; i < R; ++i)
      v[i] = __hip_atomic_load((gu32*)(base) + (seen[i] ? 0 : 32 * (lane + 64 * i)), __ATOMIC_RELAXED, __HIP_MEMORY_SCOPE_AGENT);
    bool all = true;
#pragma unroll
    for (int i = 0; i < R; ++i) { seen[i] = seen[i] || v[i] == tag; all &= seen[i]; }
    if (__all(all)) break;
    __builtin_amdgcn_s_sleep(SLEEP);
  }
}

constexpr int P0_LDS = (67 + 64) * 128;
__device__ __forceinline__ void p0_stage(LAS char* kL, const u16* __restrict__ k, const u16* __restrict__ v, int tile, int t) {
  LAS char* vL = kL + 67 * 128;
  const int s0 = (tile / 10) * 64, d0 = (tile % 10) * 64;
  const int c8 = t & 7, r = t >> 3;
  const size_t off0 = (size_t)(s0 + r) * D_ + d0 + c8 * 8, off1 = off0 + (size_t)32 * D_;
  const v4u k0 = ld_nt16(k + off0), k1 = ld_nt16(k + off1);
  const v4u v0 = ld_nt16(v + off0), v1 = ld_nt16(v + off1);
  v4u kh = {0u, 0u, 0u, 0u};
  if (t < 24 && s0 > 0) kh = ld_nt16(k + (size_t)(s0 - 3 + r) * D_ + d0 + c8 * 8);
  auto sw = [&](int row) { return row * 128 + ((c8 ^ ((row >> 3) & 7)) << 4); };
  *(LAS v4u*)(kL + sw(r + 3)) = k0;
  *(LAS v4u*)(kL + sw(r + 35)) = k1;
  *(LAS v4u*)(vL + sw(r)) = v0;
  *(LAS v4u*)(vL + sw(r + 32)) = v1;
  if (t < 24) *(LAS v4u*)(kL + sw(r)) = kh;
}
__device__ __forceinline__ void p0_compute(LAS char* kL, v4u wv, u16* __restrict__ kvT, int tile, int t) {
  LAS char* vL = kL + 67 * 128;
  const int s0 = (tile / 10) * 64, d0 = (tile % 10) * 64;
  const int rg = t & 7, p = t >> 3;
  const float wl[4] = {bf_lo(wv.x), bf_hi(wv.x), bf_lo(wv.y), bf_hi(wv.y)};
  const float wh[4] = {bf_lo(wv.z), bf_hi(wv.z), bf_lo(wv.w), bf_hi(wv.w)};
  float kl[11], kh[11];
#pragma unroll
  for (int i = 0; i < 11; ++i) {
    const int row = 8 * rg + i;
    const unsigned w = *(LAS unsigned*)(kL + row * 128 + (((p >> 2) ^ ((row >> 3) & 7)) << 4) + (p & 3) * 4);
    kl[i] = bf_lo(w); kh[i] = bf_hi(w);
  }
  float ol[8], oh[8];
#pragma unroll
  for (int i = 0; i < 8; ++i) {
    const int row = 8 * rg + i;
    const unsigned w = *(LAS unsigned*)(vL + row * 128 + (((p >> 2) ^ rg) << 4) + (p & 3) * 4);
    float sl = kl[i] * wl[3]; sl = fmaf(kl[i + 1], wl[2], sl); sl = fmaf(kl[i + 2], wl[1], sl); sl = fmaf(kl[i + 3], wl[0], sl);
    float sh = kh[i] * wh[3]; sh = fmaf(kh[i + 1], wh[2], sh); sh = fmaf(kh[i + 2], wh[1], sh); sh = fmaf(kh[i + 3], wh[0], sh);
    ol[i] = sl * bf_lo(w); oh[i] = sh * bf_hi(w);
  }
  const v4u pl = {pk_bf16(ol[0], ol[1]), pk_bf16(ol[2], ol[3]), pk_bf16(ol[4], ol[5]), pk_bf16(ol[6], ol[7])};
  const v4u ph = {pk_bf16(oh[0], oh[1]), pk_bf16(oh[2], oh[3]), pk_bf16(oh[4], oh[5]), pk_bf16(oh[6], oh[7])};
  const __amdgpu_buffer_rsrc_t r = rsrc_of(kvT, D_ * SP_ * 2);
  const unsigned off = ((d0 + 2 * p) * SP_ + s0 + 8 * rg) * 2;
  __builtin_amdgcn_raw_buffer_store_b128(pl, r, off, 0, 16);
  __builtin_amdgcn_raw_buffer_store_b128(ph, r, off + SP_ * 2, 0, 16);
}

constexpr int HC_STRIDE = 4160;
constexpr int U_STRIDE = 4576;
constexpr int K1_U_OFF = 4 * 4 * HC_STRIDE;
constexpr int K1_CONV_WG = 160;
#define AUX_DELAY 3

struct FilterRegs { v4u cur[4]; v2u prev[4]; };
__device__ __forceinline__ void conv_filter_load(FilterRegs& f, const u16* __restrict__ ssm_k, int d, int lane) {
  const u16* hrow = ssm_k + (size_t)d * S_;
#pragma unroll
  for (int it = 0; it < 4; ++it) {
    const int cI = lane + 64 * it;
    f.cur[it] = ld_nt16(hrow + 8 * cI);
    f.prev[it] = *(const v2u*)(hrow + (cI > 0 ? 8 * cI - 4 : 0));
  }
}
__device__ __forceinline__ void conv_filter_build(const FilterRegs& f, LAS char* lds, int wave, int lane) {
  LAS char* hc = lds + wave * (4 * HC_STRIDE);
  LAS char* uL = lds + K1_U_OFF + wave * U_STRIDE;
  const v4u z4 = {0u, 0u, 0u, 0u};
  if (lane < 30) *(LAS v4u*)(uL + lane * 16) = z4;
  if (lane < 8) *(LAS v4u*)(hc + (lane >> 1) * HC_STRIDE + (lane & 1) * 16) = z4;
#pragma unroll
  for (int it = 0; it < 4; ++it) {
    const int cI = lane + 64 * it;
    unsigned p0 = f.prev[it].x, p1 = f.prev[it].y;
    if (it == 0) { p0 = cI > 0 ? p0 : 0u; p1 = cI > 0 ? p1 : 0u; }
    const v4u c = f.cur[it];
    const unsigned om = __builtin_amdgcn_alignbit(p1, p0, 16);
    const unsigned o0 = __builtin_amdgcn_alignbit(c.x, p1, 16);
    const unsigned o1 = __builtin_amdgcn_alignbit(c.y, c.x, 16);
    const unsigned o2 = __builtin_amdgcn_alignbit(c.z, c.y, 16);
    const unsigned o3 = __builtin_amdgcn_alignbit(c.w, c.z, 16);
    LAS char* dst = hc + 32 + cI * 16;
    *(LAS v4u*)(dst) = c;
    *(LAS v4u*)(dst + HC_STRIDE) = v4u{o0, o1, o2, o3};
    *(LAS v4u*)(dst + 2 * HC_STRIDE) = v4u{p1, c.x, c.y, c.z};
    *(LAS v4u*)(dst + 3 * HC_STRIDE) = v4u{om, o0, o1, o2};
  }
}
__device__ __forceinline__ void conv_run(LAS char* lds, const u16* __restrict__ kvT, float Dd, u16* __restrict__ y2T,
                                         int d, int wave, int lane STAMP_PARAM) {
  LAS char* hc = lds + wave * (4 * HC_STRIDE);
  LAS char* uL = lds + K1_U_OFF + wave * U_STRIDE;
  {
    const __amdgpu_buffer_rsrc_t r = rsrc_of(kvT, D_ * SP_ * 2);
    v4u u[4];
#pragma unroll
    for (int j = 0; j < 4; ++j) u[j] = __builtin_amdgcn_raw_buffer_load_b128(r, d * (SP_ * 2) + j * 1024 + lane * 16, 0, 16);
#pragma unroll
    for (int j = 0; j < 4; ++j) *(LAS v4u*)(uL + 480 + j * 1024 + lane * 16) = u[j];
  }
  WAIT_LGKM0();
  const int g = lane >> 4, n = lane & 15, qq = n >> 2, p = n & 3;
  LAS char* aBase = hc + qq * HC_STRIDE + 56 - 16 * g + 8 * p;
  LAS char* bBase = uL + 32 * n + 16 * g;
  f32x4 acc[8];
#pragma unroll
  for (int t = 0; t < 8; ++t) acc[t] = f32x4{0.f, 0.f, 0.f, 0.f};
  v4u A[2][8], B[2][8];
#pragma unroll
  for (int a = 0; a < 8; ++a) {
    B[0][a] = *(LAS v4u*)(bBase + 448 + 512 * a);
    A[0][a] = tr_read2(aBase + 512 * a + 8, aBase + 512 * a);
  }
  SCHED_FENCE();

  unsigned aA = lds_addr_of(aBase + 64), bA = lds_addr_of(bBase + 448 - 128);
  asm volatile("" : "+v"(aA), "+v"(bA));
  LAS char* aP = (LAS char*)(size_t)aA;
  LAS char* bP = (LAS char*)(size_t)bA;
#pragma unroll CONV_UNROLL
  for (int r2 = 0; r2 < 4; ++r2) {
#pragma unroll
    for (int h = 0; h < 2; ++h) {
#pragma unroll
      for (int a = 0; a < 8; ++a) {
        B[h ^ 1][a] = *(LAS v4u*)(bP + 64 * (1 - h) + 512 * a);
        A[h ^ 1][a] = tr_read2(aP + 64 * h + 512 * a + 8, aP + 64 * h + 512 * a);
      }
#pragma unroll
      for (int b = 0; b < 8; ++b)
#pragma unroll
        for (int a = 0; a < 8 - b; ++a) acc[a + b] = mfma16(A[h][a], B[h][b], acc[a + b]);
#pragma unroll
      for (int i = 0; i < 24; ++i) { SGB(SG_MFMA, 1); SGB(SG_DS_R, 1); }
      SGB(SG_MFMA, 12);
      SCHED_FENCE();
    }
    aP += 128;
    bP -= 128;
  }
  CSTAMP(0, 5);
#pragma unroll
  for (int t = 0; t < 8; ++t) {
    LAS v2u* up = (LAS v2u*)(uL + 480 + 512 * t + 32 * n + 8 * g);
    const v2u u = *up;
    const float y0 = fmaf(bf_lo(u.x), Dd, acc[t][0]), y1 = fmaf(bf_hi(u.x), Dd, acc[t][1]);
    const float y2 = fmaf(bf_lo(u.y), Dd, acc[t][2]), y3 = fmaf(bf_hi(u.y), Dd, acc[t][3]);
    *up = v2u{pk_bf16(y0, y1), pk_bf16(y2, y3)};
  }
  const __amdgpu_buffer_rsrc_t yr = rsrc_of(y2T, D_ * SP_ * 2);
#pragma unroll
  for (int j = 0; j < 4; ++j)
    __builtin_amdgcn_raw_buffer_store_b128(*(LAS v4u*)(uL + 480 + 1024 * j + 16 * lane), yr, d * (SP_ * 2) + j * 1024 + lane * 16, 0, 16);
}

struct ModRegs { unsigned cv[5]; v4u w[5]; };
__device__ __forceinline__ void mod_load(ModRegs& m, const u16* __restrict__ c, const u16* __restrict__ w_mod, int task, int t) {
  const int cb = task >> 2, kq = task & 3, cc = t & 7, rg = t >> 3;
  const u16* cp = c + 160 * kq + rg;
  const u16* wp = w_mod + (size_t)(160 * kq + rg) * 3840 + cb * 64 + cc * 8;
#pragma unroll
  for (int i = 0; i < 5; ++i) asm volatile("global_load_ushort %0, %1, off" : "=v"(m.cv[i]) : "v"(cp + 32 * i) : "memory");
#pragma unroll
  for (int i = 0; i < 5; ++i) asm volatile("global_load_dwordx4 %0, %1, off nt" : "=v"(m.w[i]) : "v"(wp + (size_t)i * 32 * 3840) : "memory");
}
__device__ __forceinline__ float fold_8_16_32(float v) {
  v += dpp_ror8(v);
  { const auto r = __builtin_amdgcn_permlane16_swap(__float_as_uint(v), __float_as_uint(v), false, false); v = __uint_as_float(r[0]) + __uint_as_float(r[1]); }
  { const auto r = __builtin_amdgcn_permlane32_swap(__float_as_uint(v), __float_as_uint(v), false, false); v = __uint_as_float(r[0]) + __uint_as_float(r[1]); }
  return v;
}
__device__ __forceinline__ void mod_finish(ModRegs& m, LAS char* red_, LAS char* sy, int sy_word, char* __restrict__ ws, int task, unsigned tag, int t) {
  const int cb = task >> 2, kq = task & 3;
  const int cc = t & 7, lane = t & 63;
  asm volatile("s_waitcnt vmcnt(0)"
               : "+v"(m.cv[0]), "+v"(m.cv[1]), "+v"(m.cv[2]), "+v"(m.cv[3]), "+v"(m.cv[4]),
                 "+v"(m.w[0]), "+v"(m.w[1]), "+v"(m.w[2]), "+v"(m.w[3]), "+v"(m.w[4]) :: "memory");
  float cv[5];
#pragma unroll
  for (int i = 0; i < 5; ++i) cv[i] = bf_lo(m.cv[i]);
  const v4u (&w)[5] = m.w;
  float acc[8];
#pragma unroll
  for (int q = 0; q < 8; ++q) acc[q] = 0.f;
#pragma unroll
  for (int i = 0; i < 5; ++i) {
    const float sv = rnd_bf16(cv[i] * __builtin_amdgcn_rcpf(1.f + __expf(-cv[i])));
    acc[0] = fmaf(sv, bf_lo(w[i].x), acc[0]); acc[1] = fmaf(sv, bf_hi(w[i].x), acc[1]);
    acc[2] = fmaf(sv, bf_lo(w[i].y), acc[2]); acc[3] = fmaf(sv, bf_hi(w[i].y), acc[3]);
    acc[4] = fmaf(sv, bf_lo(w[i].z), acc[4]); acc[5] = fmaf(sv, bf_hi(w[i].z), acc[5]);
    acc[6] = fmaf(sv, bf_lo(w[i].w), acc[6]); acc[7] = fmaf(sv, bf_hi(w[i].w), acc[7]);
  }
#pragma unroll
  for (int q = 0; q < 8; ++q) acc[q] = fold_8_16_32(acc[q]);
  LAS float* red = (LAS float*)red_;
  if (lane < 8) {
    *(LAS f32x4*)(red + (t >> 6) * 64 + cc * 8) = f32x4{acc[0], acc[1], acc[2], acc[3]};
    *(LAS f32x4*)(red + (t >> 6) * 64 + cc * 8 + 4) = f32x4{acc[4], acc[5], acc[6], acc[7]};
  }
  WAIT_LGKM0();
  if (lds_arrive(sy, sy_word, lane) == 3) {
    if (lane < 16) {
      const f32x4 sum = (*(LAS f32x4*)(red + 4 * lane) + *(LAS f32x4*)(red + 64 + 4 * lane)) +
                        (*(LAS f32x4*)(red + 128 + 4 * lane) + *(LAS f32x4*)(red + 192 + 4 * lane));
      __builtin_amdgcn_raw_buffer_store_b128(__builtin_bit_cast(v4u, sum), rsrc_of(ws + WS_MOD, 4 * 3840 * 4), (kq * 3840 + cb * 64 + 4 * lane) * 4, 0, 16);
    }
    WAIT_VM(0);
    if (lane == 0) flag_store(ws + WS_FM, task, tag);
  }
}

constexpr int TILE_ITEMS = 540 + 108;
__device__ __forceinline__ void tile_load(LAS char* buf, const u16* __restrict__ w1, const u16* __restrict__ w2, int item, int lane) {
  const u16* src; int ld;
  if (item < 540) { src = w1 + (size_t)(32 * (item / 27)) * F_ + 64 * (item % 27); ld = F_; }
  else { const int it = item - 540; src = w2 + (size_t)(32 * (it >> 1)) * D_ + 512 + 64 * (it & 1); ld = D_; }
#pragma unroll
  for (int j = 0; j < 4; ++j) GLDS16_NT(src + (size_t)(8 * j + (lane >> 3)) * ld + (lane & 7) * 8, buf + j * 1024);
}
__device__ __forceinline__ void tile_store(LAS char* buf, u16* __restrict__ w1t, int item, int lane) {
  const int g = lane >> 4, q = (lane & 15) >> 2, p = lane & 3;
  LAS char* rowp = buf + (8 * g + q) * 128;
#pragma unroll
  for (int PT = 0; PT < 4; ++PT) {
    unsigned dst; int fo;
    if (item < 540) {
      const int kk = item / 27, fg = item % 27;
      fo = 32 * (PT >> 1) + 8 * p + 4 * (PT & 1);
      dst = (((2 * fg + (PT >> 1)) * 20 + kk) * 2 + (PT & 1)) * 1024;
    } else {
      const int it = item - 540;
      fo = 16 * PT + 4 * p;
      dst = D_ * F_ * 2 + ((4 * (it & 1) + PT) * 54 + (it >> 1)) * 1024;
    }
    const v4u f = tr_read2(rowp + 2 * fo, rowp + 2 * fo + 4 * 128);
    __builtin_amdgcn_raw_buffer_store_b128(f, rsrc_of(w1t, (D_ + 128) * F_ * 2), dst + lane * 16, 0, 16);
  }
}

constexpr int K1_TILE_OFF = K1_U_OFF + 4 * U_STRIDE;
constexpr int K1_LDS = K1_TILE_OFF + 4 * 4096;

constexpr int K2_CST_OFF = K1_TILE_OFF;
constexpr int K2_TAB_OFF = 153600;
constexpr int K2_TAB_ONE = 1408, K2_TAB_ZERO = 2752;
constexpr int K2_RED_OFF = K2_TAB_OFF + K2_TAB_ZERO + 1280;
constexpr int K2_R3_OFF = K2_RED_OFF + 64;
constexpr int K3_EPI_OFF = K2_R3_OFF + 256;
constexpr int SYNC_OFF = K2_R3_OFF + 1024;
constexpr int XST_OFF = 101632;
enum { SY_TILE_LO = 0, SY_TILE_HI = 1, SY_CONV = 2, SY_CONSTS = 3, SY_XREADY = 4  , SY_WTILE = 8, SY_F0 = 9, SY_FM = 10, SY_MODRED = 11, SY_LOOPDONE = 12, SY_HOK = 13,
       SY_W2READY = 16  , SY_R3 = 25, SY_DUMMY = 27,
       SY_W2FREE = 32  ,
       SY_KA = 48  , SY_KB = 56  , SY_WORDS = 64 };

struct RowStat { float rs, nm; };

__device__ __forceinline__ float row_sumsq(const f32x4& d2, int lane) {
  const int n = lane & 15;
  const float dg = (n & 2) ? ((n & 1) ? d2[3] : d2[2]) : ((n & 1) ? d2[1] : d2[0]);
  const float sq = __int_as_float(__builtin_amdgcn_ds_bpermute((n + 16 * (n >> 2)) * 4, __float_as_int(dg)));
  return sq + dpp_ror8(sq);
}
__device__ __forceinline__ RowStat finish_stats(float sm, float sq) {
  const float mean = sm * (1.f / D_);
  const float var = fmaxf(sq * (1.f / D_) - mean * mean, 0.f);
  const float rs = __builtin_amdgcn_rsqf(var + LN_EPS);
  return RowStat{rs, -mean * rs};
}
constexpr unsigned ONES2 = 0x3f803f80u;

__device__ __forceinline__ f32x2 gelu_tanh2(f32x2 x) {
  constexpr float C0 = -2.f * 0.7978845608028654f * 1.4426950408889634f;
  constexpr float C1 = C0 * 0.044715f;
  const f32x2 c0 = {C0, C0}, c1 = {C1, C1}, one = {1.f, 1.f};
  const f32x2 t = x * __builtin_elementwise_fma(x * x, c1, c0);
  const f32x2 d = f32x2{__builtin_amdgcn_exp2f(t[0]), __builtin_amdgcn_exp2f(t[1])} + one;
  return x * f32x2{__builtin_amdgcn_rcpf(d[0]), __builtin_amdgcn_rcpf(d[1])};
}

constexpr int K3_STAGE = 6 * 5 * 1024;
__host__ __device__ constexpr int k3_slot(int s) { return ((s % 5) * K3_STAGE + 3 * K3_STAGE) % (5 * K3_STAGE); }
constexpr int K23_LDS = SYNC_OFF + 256;
static_assert(k3_slot(0) >= 81920 && k3_slot(1) + K3_STAGE <= K2_TAB_OFF && K2_CST_OFF + 10240 <= XST_OFF &&
              XST_OFF + 4 * 10240 <= K2_TAB_OFF && K3_EPI_OFF + 640 <= SYNC_OFF && K23_LDS <= 163840, "LDS map");
#define K3_PF 7
#define K3_ROT 0
#define W2_L2PREF 0
#define W2_L2PREF_WAVE 7

__global__ __launch_bounds__(512) void adaln_block(const u16* __restrict__ x, const u16* __restrict__ kin, const u16* __restrict__ vin,
                                                   const u16* __restrict__ shift_k, const u16* __restrict__ ssm_k, const u16* __restrict__ ssm_D,
                                                   const u16* __restrict__ w1, const u16* __restrict__ b1,
                                                   const u16* __restrict__ w2, const u16* __restrict__ b2,
                                                   const u16* __restrict__ cvec, const u16* __restrict__ w_mod,
                                                   u16* __restrict__ out, char* __restrict__ ws STAMP_ARG) {
  extern __shared__ __attribute__((aligned(16))) char lds_dyn[];
  LAS char* lds = (LAS char*)lds_dyn;
  STAMP_DECL
  STAMP(0, 0);
  const int tid = threadIdx.x, lane = tid & 63, wave = __builtin_amdgcn_readfirstlane(tid >> 6);
  const int b = blockIdx.x;
  u16* kvT = (u16*)(ws + WS_KVT);
  u16* y2T = (u16*)(ws + WS_Y2T);
  u16* x2g = (u16*)(ws + WS_X2);
  u16* Ht = (u16*)(ws + WS_HT);
  u16* w1t = (u16*)(ws + WS_W1T);
  float* mod = (float*)(ws + WS_MOD);
  asm volatile("" :: "s"(kin), "s"(vin), "s"(shift_k), "s"(ws), "s"(ssm_k), "s"(ssm_D), "s"(cvec), "s"(w_mod));
  unsigned seed;
  asm volatile("global_load_dword %0, %1, %2 sc1" : "=v"(seed) : "v"(0u), "s"(ws + WS_TAG) : "memory");

  LAS char* sy = lds + SYNC_OFF;
  if (tid < SY_WORDS) ((LAS unsigned*)sy)[tid] = 0u;
  const bool conv = b < K1_CONV_WG;
  const int j = b - K1_CONV_WG;
  const bool upper = wave >= 4;
  const bool tile_wave = conv ? upper : j < 80;
  const int tile = conv ? b : K1_CONV_WG + 2 * j + (wave >> 2);
  LAS char* stage = lds + (upper ? K1_TILE_OFF : 0);
  v4u wv;
  if (tile_wave) {
    wv = *(const v4u*)(shift_k + (size_t)((tile % 10) * 64 + 2 * ((tid & 255) >> 3)) * 4);
    p0_stage(stage, kin, vin, tile, tid & 255);
  }
  asm volatile("s_waitcnt vmcnt(0) lgkmcnt(0)" : "+v"(seed) :: "memory");
  const unsigned tag = __builtin_amdgcn_readfirstlane(seed) + 0x9E3779B9u;
  BARRIER();
  CSTAMP(0, 2);
  const bool mod_wave = upper && b < 240;
  ModRegs mreg;
  if (tile_wave) {
    p0_compute(stage, wv, kvT, tile, tid & 255);
    if (mod_wave) { mod_load(mreg, cvec, w_mod, b, tid & 255); WAIT_VM(10); } else WAIT_VM(0);
    if (lds_arrive(sy, upper ? SY_TILE_HI : SY_TILE_LO, lane) == 3 && lane == 0)
      flag_store(ws + WS_F0, 32 * (tile % 10) + tile / 10, tag);
  }
  if (conv && !upper) {
    const int d = 4 * b + wave;
    FilterRegs filt;
    conv_filter_load(filt, ssm_k, d, lane);
    const float Dd = bf_lo((unsigned)ssm_D[d]);
    if (tid == 0) flag_store(ws + WS_FT, b, tag);
    conv_filter_build(filt, lds, wave, lane);
    CSTAMP(0, 3);
    if (wave == 0) { poll_flags<32, SL0>(ws + WS_F0 + 32 * 128 * (b >> 4), tag, lane); lds_signal(sy, SY_F0, lane); }
    else lds_wait(sy, SY_F0, 1);
    CSTAMP(0, 4);
    conv_run(lds, kvT, Dd, y2T, d, wave, lane STAMP_FWD);
    WAIT_VM(0);
    if (lds_arrive(sy, SY_CONV, lane) == 3 && lane == 0) flag_store(ws + WS_F1, b, tag);
    CSTAMP(0, 6);
  } else {
    if (mod_wave) {
      __builtin_amdgcn_s_setprio(2);
      mod_finish(mreg, lds + K2_R3_OFF, sy, SY_MODRED, ws, b, tag, tid & 255);
      __builtin_amdgcn_s_setprio(0);
    }
    CSTAMP(0, 3);
    if (!conv && !upper) {
      const int gw = 4 * j + wave, i1 = gw + 384;
      if (j < 80) lds_wait(sy, SY_TILE_LO, 4);
      for (int i = 0; i < TILE_DELAY; ++i) __builtin_amdgcn_s_sleep(16);
      LAS char* buf = lds + wave * 8192;
      tile_load(buf, w1, w2, gw, lane);
      if (i1 < TILE_ITEMS) tile_load(buf + 4096, w1, w2, i1, lane);
      WAIT_VM(0);
      tile_store(buf, w1t, gw, lane);
      if (i1 < TILE_ITEMS) tile_store(buf + 4096, w1t, i1, lane);
      WAIT_VM(0);
      if (lds_arrive(sy, SY_WTILE, lane) == 3 && lane == 0) flag_store(ws + WS_FT, b, tag);
      const int l = gw * 36 + lane;
      if (lane < 36) { const unsigned t = *(const unsigned*)((const char*)w2 + (size_t)(l >> 3) * 1280 + (l & 7) * 128); asm volatile("" :: "v"(t)); }
    }
  }
  const int xl = b & 7, mI = b >> 3;
  const int rt = xl + 8 * (mI >> 3), cg8 = mI & 7;
  const int P0 = cg8 < 6 ? 7 * cg8 : 42 + 6 * (cg8 - 6);
  const int np = cg8 < 6 ? 7 : 6;
  const int r = lane & 7, cg = lane >> 3;
  const unsigned l0 = lds_addr_of(lds);
  const __amdgpu_buffer_rsrc_t hrs = rsrc_of(Ht, S_ * F_ * 2);
  int rot = K3_ROT ? cg8 + 2 * (mI >> 3) : 0;
  rot = rot >= 9 ? rot - 9 : rot;
  unsigned w2vb;
  {
    const int rl = lane >> 3, f = (rl >> 1) & 1;
    w2vb = rl * 1280 + (((lane & 7) ^ (f << 1)) << 4);
  }
  auto w2issue = [&](int s, int ks0, int ks1, int pace) {
    int sr = s + rot; sr = sr >= 9 ? sr - 9 : sr;
#pragma unroll
    for (int ks = ks0; ks < ks1; ++ks) {
      for (int q = 0; q < pace; ++q) __builtin_amdgcn_s_sleep(1);
      const char* sp = (const char*)w2 + (size_t)(32 * (6 * sr + ks)) * 1280;
      const unsigned dp = l0 + k3_slot(s) + ks * 5120;
#pragma unroll
      for (int i = 0; i < 4; ++i) glds16_s(w2vb ^ ((i & 1) << 6), sp + i * (8 * 1280) + 128 * cg8, dp + i * 1024);
      glds16_s_sc1(lane * 16, ws + WS_W2T + (size_t)(cg8 * 54 + 6 * sr + ks) * 1024, dp + 4096);
    }
  };
  auto w2park = [&](int s, v4u (&pk)[30], int pace) {
    int sr = s + rot; sr = sr >= 9 ? sr - 9 : sr;
    static_for<0, 30>([&](auto c) {
      constexpr int ks = c / 5, i = c % 5;
      if constexpr (i == 0) { for (int q = 0; q < pace; ++q) __builtin_amdgcn_s_sleep(1); }
      if constexpr (i < 4) gld16s<0>(pk[c], w2vb ^ ((i & 1) << 6), (const char*)w2 + (size_t)(32 * (6 * sr + ks) + 8 * i) * 1280 + 128 * cg8);
      else gld16s_sc1<0>(pk[c], lane * 16, ws + WS_W2T + (size_t)(cg8 * 54 + 6 * sr + ks) * 1024);
    });
  };
  const unsigned slot = l0 + XST_OFF + (wave & 3) * 10240;
  auto xissue = [&](int w) {
    const char* xs = (const char*)(x + (size_t)(64 * rt + 8 * w) * D_);
#pragma unroll
    for (int i = 0; i < 10; ++i) {
      const int c = 64 * i + lane, rr = c / 80, pos = c - 80 * rr;
      glds16_s(rr * 1280 + ((pos ^ (2 * rr)) << 4), xs, slot + i * 1024);
    }
  };
  if (upper) { xissue(wave); WAIT_VM(0); }
  else lds_wait(sy, SY_XREADY + wave, 1);
  v4u xw[10];
  {
    LAS char* xl_ = lds + XST_OFF + (wave & 3) * 10240 + r * 1280;
#pragma unroll
    for (int m = 0; m < 10; ++m) xw[m] = *(LAS v4u*)(xl_ + (((8 * m + cg) ^ (2 * r)) << 4));
  }
  asm volatile("s_waitcnt lgkmcnt(0)"
               : "+v"(xw[0]), "+v"(xw[1]), "+v"(xw[2]), "+v"(xw[3]), "+v"(xw[4]), "+v"(xw[5]), "+v"(xw[6]), "+v"(xw[7]), "+v"(xw[8]), "+v"(xw[9])
               :: "memory");
  if (upper) xissue(wave - 4);
  CSTAMP(0, 7);
  RowStat s1;
  {
    const v4u ones = {ONES2, ONES2, ONES2, ONES2};
    f32x4 d1 = {0.f, 0.f, 0.f, 0.f}, d2 = {0.f, 0.f, 0.f, 0.f};
#pragma unroll
    for (int m = 0; m < 10; ++m) { d1 = mfma16(ones, xw[m], d1); d2 = mfma16(xw[m], xw[m], d2); }
    s1 = finish_stats(d1[0] + dpp_ror8(d1[0]), row_sumsq(d2, lane));
  }
  if (upper) { WAIT_VM(0); lds_signal(sy, SY_XREADY + wave - 4, lane); }
  LAS float* cst = (LAS float*)(lds + K2_CST_OFF);
  if (wave >= 4 && wave < 7) {
      if (conv || j < 80) lds_wait(sy, SY_TILE_HI, 4);
      if (wave == 4) { poll_flags<240, SLM>(ws + WS_FM, tag, lane); lds_signal(sy, SY_FM, lane); }
      else lds_wait(sy, SY_FM, 1);
      const int t = tid - 256;
      const char* mp = (const char*)mod;
      const unsigned mo = 16 * (t < 160 ? t : 159);
      f32x4 mv[5];
      auto batch = [&](auto j0c, auto nc) {
        constexpr int j0 = j0c, n = nc;
        v4u md[3][4];
        static_for<0, 4 * n>([&](auto ic) { constexpr int i = ic, jj = i / 4, kq = i % 4; gld16s_sc1<0>(md[jj][kq], mo, mp + (kq * 3840 + (j0 + jj) * D_) * 4); });
        static_for<0, n>([&](auto jc) {
          constexpr int jj = jc;
          asm volatile("s_waitcnt vmcnt(%c4)" : "+v"(md[jj][0]), "+v"(md[jj][1]), "+v"(md[jj][2]), "+v"(md[jj][3]) : "i"(4 * (n - 1 - jj)) : "memory");
          const f32x4 sm4 = (__builtin_bit_cast(f32x4, md[jj][0]) + __builtin_bit_cast(f32x4, md[jj][1])) +
                            (__builtin_bit_cast(f32x4, md[jj][2]) + __builtin_bit_cast(f32x4, md[jj][3]));
#pragma unroll
          for (int q = 0; q < 4; ++q) mv[j0 + jj][q] = rnd_bf16(sm4[q]);
        });
      };
      batch(std::integral_constant<int, 0>{}, std::integral_constant<int, 3>{});
      batch(std::integral_constant<int, 3>{}, std::integral_constant<int, 2>{});
      const f32x4 sa = mv[0], ca = mv[1], ga = mv[2], sb = mv[3], cb = mv[4];
      f32x4 g1, a2;
#pragma unroll
      for (int q = 0; q < 4; ++q) { g1[q] = ga[q] * rnd_bf16(1.f + ca[q]); a2[q] = rnd_bf16(1.f + cb[q]); }
      float pa = 0.f, ps = 0.f;
      if (t < 160) {
        *(LAS f32x4*)(cst + 4 * t) = g1;
        *(LAS f32x4*)(cst + D_ + 4 * t) = ga * sa;
        *(LAS f32x4*)(cst + 2 * D_ + 4 * t) = a2;
        *(LAS f32x4*)(cst + 3 * D_ + 4 * t) = sb;
        *(LAS v2u*)(lds + K2_TAB_OFF + 8 * t) = v2u{pk_bf16(a2[0], a2[1]), pk_bf16(a2[2], a2[3])};
        *(LAS v2u*)(lds + K2_TAB_OFF + K2_TAB_ONE + 8 * t) = v2u{ONES2, ONES2};
        *(LAS v2u*)(lds + K2_TAB_OFF + K2_TAB_ZERO + 8 * t) = v2u{0u, 0u};
        pa = (a2[0] + a2[1]) + (a2[2] + a2[3]);
        ps = (sb[0] + sb[1]) + (sb[2] + sb[3]);
      }
      {
        auto wsum = [](float v) {
          v += __int_as_float(__builtin_amdgcn_update_dpp(0, __float_as_int(v), 0xb1, 0xf, 0xf, false));
          v += __int_as_float(__builtin_amdgcn_update_dpp(0, __float_as_int(v), 0x4e, 0xf, 0xf, false));
          v += __int_as_float(__builtin_amdgcn_update_dpp(0, __float_as_int(v), 0x124, 0xf, 0xf, false));
          return fold_8_16_32(v);
        };
        pa = wsum(pa); ps = wsum(ps);
      }
      if (lane == 0) *(LAS f32x2*)(lds + K2_RED_OFF + 8 * (wave - 4)) = f32x2{pa, ps};
      WAIT_LGKM0();
      lds_signal(sy, SY_CONSTS, lane);
  }
  f32x2 val[10][4], xf[10][4];
  {
    const f32x2 rs2 = {s1.rs, s1.rs}, nm2 = {s1.nm, s1.nm};
#pragma unroll
    for (int m = 0; m < 10; ++m) {
#pragma unroll
      for (int q = 0; q < 4; ++q) {
        xf[m][q] = f32x2{bf_lo(xw[m][q]), bf_hi(xw[m][q])};
        val[m][q] = __builtin_elementwise_fma(xf[m][q], rs2, nm2);
      }
      asm volatile("" : "+v"(val[m][0]), "+v"(val[m][1]), "+v"(val[m][2]), "+v"(val[m][3]),
                        "+v"(xf[m][0]), "+v"(xf[m][1]), "+v"(xf[m][2]), "+v"(xf[m][3]));
    }
  }
  LAS float* cl = cst + 8 * cg;
  lds_wait(sy, SY_CONSTS, 3);
  {
    f32x4 c0[2][2], c1[2][2];
    c0[0][0] = *(LAS f32x4*)(cl); c0[0][1] = *(LAS f32x4*)(cl + 4);
    c1[0][0] = *(LAS f32x4*)(cl + D_); c1[0][1] = *(LAS f32x4*)(cl + D_ + 4);
#pragma unroll
    for (int m = 0; m < 10; ++m) {
      const int cu = m & 1, nx = cu ^ 1;
      if (m < 9) {
        c0[nx][0] = *(LAS f32x4*)(cl + 64 * (m + 1)); c0[nx][1] = *(LAS f32x4*)(cl + 64 * (m + 1) + 4);
        c1[nx][0] = *(LAS f32x4*)(cl + D_ + 64 * (m + 1)); c1[nx][1] = *(LAS f32x4*)(cl + D_ + 64 * (m + 1) + 4);
      }
      SCHED_FENCE();
#pragma unroll
      for (int q = 0; q < 4; ++q) {
        const f32x2 G1 = {c0[cu][q >> 1][2 * (q & 1)], c0[cu][q >> 1][2 * (q & 1) + 1]};
        const f32x2 G2 = {c1[cu][q >> 1][2 * (q & 1)], c1[cu][q >> 1][2 * (q & 1) + 1]};
        val[m][q] = __builtin_elementwise_fma(val[m][q], G1, G2);
      }
      asm volatile("" : "+v"(val[m][0]), "+v"(val[m][1]), "+v"(val[m][2]), "+v"(val[m][3]));
      SCHED_FENCE();
    }
  }
  CSTAMP(0, 8);
  if (wave == 0) poll_flags<K1_CONV_WG, SL1>(ws + WS_F1, tag, lane);
  else if (wave == 1) poll_flags<256, SL1>(ws + WS_FT, tag, lane);
  WAIT_LGKM0();
  BARRIER();
  CSTAMP(0, 9);
  {
    const int chl = lane >> 3, pos = lane & 7;
    const unsigned vb = chl * (SP_ * 2), pa = pos ^ ((chl >> 1) & 1);
#pragma unroll
    for (int i = 0; i < 10; ++i) {
      const int I = 8 * i + wave;
      const unsigned voff = vb + ((pa ^ ((I & 3) << 1)) << 4);
      glds16_s_sc1(voff, (const char*)(y2T + (size_t)(8 * I) * SP_ + 64 * rt), l0 + I * 1024);
    }
  }
  wait_vm_only<10 - YG>();
  BARRIER();
  if (b == 0 && tid == 0) flag_store(ws + WS_TAG, 0, tag);
  CSTAMP(0, 10);
  constexpr int PFB = 5, PF = G1_WIN;
  const int P = P0 + (wave < np ? wave : np - 1);
  const char* wp = (const char*)w1t + (size_t)P * 40 * 1024;
  const unsigned wl = lane * 16;
  v4u wa[20][2];
  f32x4 d1 = {0.f, 0.f, 0.f, 0.f}, d2 = {0.f, 0.f, 0.f, 0.f};
  {
    const int G = lane >> 4, q = (lane & 15) >> 2, p = lane & 3;
    const int chb = 8 * (2 * G + (p >> 1)) + q;
    const int fb = ((chb >> 1) & 1) | (((chb >> 3) & 1) << 1) | (((chb >> 4) & 1) << 2);
    const int fc = (((chb + 4) >> 1) & 1) | ((((chb + 4) >> 3) & 1) << 1) | ((((chb + 4) >> 4) & 1) << 2);
    LAS char* a0 = lds + chb * 128 + ((wave ^ fb) << 4) + 8 * (p & 1);
    LAS char* a1 = lds + (chb + 4) * 128 + ((wave ^ fc) << 4) + 8 * (p & 1);
    const int ty = lane & 3;
    LAS char* st = lds + K2_TAB_OFF + (ty == 0 ? K2_TAB_ONE : ty == 3 ? K2_TAB_ZERO : 16 * (ty - 1)) + 32 * G;
    const __amdgpu_buffer_rsrc_t x2r = __builtin_amdgcn_make_buffer_rsrc(x2g, 0, S_ * D_ * 2, 0x00020000);
    const unsigned x2o = ((8 * rt + wave) * 10 * 64 + lane) * 16;
    v4u yw[10], sm[10];
    auto creads = [&](auto mc) {
      constexpr int m = mc;
      yw[m] = tr_read2(a0 + m * 8192, a1 + m * 8192); sm[m] = *(LAS v4u*)(st + m * 128);
    };
    creads(std::integral_constant<int, 0>{});
    static_for<0, 10>([&](auto mc) {
      constexpr int m = mc;
      constexpr int W0 = YW0, per = (10 + (10 - W0) - 1) / (10 - W0);
      constexpr int before = m <= W0 ? 0 : ((m - W0) * per < 10 ? (m - W0) * per : 10);
      if constexpr (m + 1 < 10) {
        if constexpr ((m + 1) % YG == 0) {
          constexpr int last = m + YG < 9 ? m + YG : 9;
          wait_vm_only<(9 - last) + before>();
          BARRIER();
        }
        creads(std::integral_constant<int, m + 1>{});
      }
      if constexpr (m >= W0) {
        static_for<0, per>([&](auto hc) {
          constexpr int f = before + hc;
          if constexpr (f < 10) gld16s_sc1<(f & 3) * 1024>(wa[f >> 1][f & 1], wl, wp + (f >> 2) * 4096);
        });
      }
      SCHED_FENCE();
      v4u pk;
#pragma unroll
      for (int q = 0; q < 4; ++q) {
        const f32x2 yf = {bf_lo(yw[m][q]), bf_hi(yw[m][q])};
        val[m][q] = __builtin_elementwise_fma(yf, val[m][q], xf[m][q]);
        pk[q] = pk_bf16(val[m][q][0], val[m][q][1]);
      }
      d1 = mfma16(sm[m], pk, d1);
      d2 = mfma16(pk, pk, d2);
      if (cg8 == (m < 5 ? 6 : 7)) __builtin_amdgcn_raw_buffer_store_b128(pk, x2r, x2o + m * 1024, 0, 16);
      SCHED_FENCE();
    });
  }
  WAIT_LGKM0();
  BARRIER();
  CSTAMP(0, 11);
  f32x4 d3 = {0.f, 0.f, 0.f, 0.f};
  {
    f32x4 c0[2][2], c1[2][2];
    c0[0][0] = *(LAS f32x4*)(cl + 2 * D_); c0[0][1] = *(LAS f32x4*)(cl + 2 * D_ + 4);
    c1[0][0] = *(LAS f32x4*)(cl + 3 * D_); c1[0][1] = *(LAS f32x4*)(cl + 3 * D_ + 4);
    const f32x2 r0 = *(LAS f32x2*)(lds + K2_RED_OFF), r1 = *(LAS f32x2*)(lds + K2_RED_OFF + 8), r2 = *(LAS f32x2*)(lds + K2_RED_OFF + 16);
    const float meanA2 = (r0[0] + r1[0] + r2[0]) * (1.f / D_), meanS2 = (r0[1] + r1[1] + r2[1]) * (1.f / D_);
    const float sx = d1[0] + dpp_ror8(d1[0]);
    const float sxa_h = (lane & 8) ? d1[2] : d1[1];
    const float sxa = sxa_h + dpp_ror8(sxa_h);
    const RowStat s2 = finish_stats(sx, row_sumsq(d2, lane));
    const float m3 = fmaf(s2.rs * (1.f / D_), sxa, fmaf(s2.nm, meanA2, meanS2));
    const f32x2 rs2 = {s2.rs, s2.rs}, nm2 = {s2.nm, s2.nm}, m32 = {m3, m3};
    const int mt = wave >> 1, n = 8 * (wave & 1) + r, g2 = cg & 3;
    LAS char* yb = lds + (((cg >> 2) * 4 + mt) * 64 + n + 16 * g2) * 16;
    static_for<0, 10>([&](auto mc) {
      constexpr int m = mc, cu = m & 1, nx = cu ^ 1;
      static_for<0, 2>([&](auto hc) {
        constexpr int f = 2 * PFB + 2 * m + hc;
        if constexpr (f < 2 * PF) gld16s_sc1<(f & 3) * 1024>(wa[f >> 1][f & 1], wl, wp + (f >> 2) * 4096);
      });
      if constexpr (m < 9) {
        c0[nx][0] = *(LAS f32x4*)(cl + 2 * D_ + 64 * (m + 1)); c0[nx][1] = *(LAS f32x4*)(cl + 2 * D_ + 64 * (m + 1) + 4);
        c1[nx][0] = *(LAS f32x4*)(cl + 3 * D_ + 64 * (m + 1)); c1[nx][1] = *(LAS f32x4*)(cl + 3 * D_ + 64 * (m + 1) + 4);
      }
      SCHED_FENCE();
      v4u o;
#pragma unroll
      for (int q = 0; q < 4; ++q) {
        const f32x2 A2 = {c0[cu][q >> 1][2 * (q & 1)], c0[cu][q >> 1][2 * (q & 1) + 1]};
        const f32x2 S2 = {c1[cu][q >> 1][2 * (q & 1)], c1[cu][q >> 1][2 * (q & 1) + 1]};
        const f32x2 h = __builtin_elementwise_fma(__builtin_elementwise_fma(val[m][q], rs2, nm2), A2, S2 - m32);
        o[q] = pk_bf16(h[0], h[1]);
      }
      *(LAS v4u*)(yb + m * 8192) = o;
      d3 = mfma16(o, o, d3);
      SCHED_FENCE();
    });
  }
  WAIT_LGKM0();
  BARRIER();
  CSTAMP(0, 12);
  {
    const float r3 = __builtin_amdgcn_rsqf(row_sumsq(d3, lane) * (1.f / D_) + LN_EPS);
    if (lane < 8) *(LAS float*)(lds + K2_R3_OFF + 4 * (8 * wave + lane)) = r3;
    lds_signal(sy, SY_R3, lane);
  }
  auto park_dump = [&](v4u (&park)[30], int slot_off, bool paced) {
    asm volatile("s_waitcnt vmcnt(0)"
                 : "+v"(park[0]), "+v"(park[1]), "+v"(park[2]), "+v"(park[3]), "+v"(park[4]), "+v"(park[5]), "+v"(park[6]), "+v"(park[7]),
                   "+v"(park[8]), "+v"(park[9]), "+v"(park[10]), "+v"(park[11]), "+v"(park[12]), "+v"(park[13]), "+v"(park[14]) :: "memory");
    asm volatile("" : "+v"(park[15]), "+v"(park[16]), "+v"(park[17]), "+v"(park[18]), "+v"(park[19]), "+v"(park[20]), "+v"(park[21]), "+v"(park[22]),
                      "+v"(park[23]), "+v"(park[24]), "+v"(park[25]), "+v"(park[26]), "+v"(park[27]), "+v"(park[28]), "+v"(park[29]) :: "memory");
    LAS char* dst = lds + slot_off + fresh_lane() * 16;
#pragma unroll
    for (int c = 0; c < 30; ++c) {
      *(LAS v4u*)(dst + c * 1024) = park[c];
      if (DUMP_PACE && paced && (c & 1)) { SCHED_FENCE(); __builtin_amdgcn_s_sleep(DUMP_PACE); SCHED_FENCE(); }
    }
    WAIT_LGKM0();
  };
  if (wave == 7) {
    WAIT_VM(0);
    v4u park[30], park2[30];
    w2issue(0, 0, 6, W2_PACE); w2issue(1, 0, 6, W2_PACE);
    w2park(4, park, W2_PACE);
    lds_wait8(sy, SY_KA, np); w2issue(2, 0, 6, W2_PACE);
    w2park(5, park2, W2_PACE);
    lds_wait8(sy, SY_KB, np); w2issue(3, 0, 6, 0);
    BARRIER();
    park_dump(park, k3_slot(4), false);
    lds_signal(sy, SY_W2READY + 2, lane); lds_signal(sy, SY_W2READY + 3, lane); lds_signal(sy, SY_W2READY + 4, lane);
    lds_wait4(sy, SY_W2FREE + 0);
    park_dump(park2, k3_slot(5), true);
    lds_signal(sy, SY_W2READY + 5, lane);
    return;
  }
  if (wave < np) {
    f32x4 acc[4][2];
#pragma unroll
    for (int mt = 0; mt < 4; ++mt) { acc[mt][0] = f32x4{0.f, 0.f, 0.f, 0.f}; acc[mt][1] = f32x4{0.f, 0.f, 0.f, 0.f}; }
    const v4u bias = *(const v4u*)(b1 + 32 * P + 8 * (lane >> 4));
    LAS char* bl = lds + lane * 16;
    constexpr int BD = G1_BD;
    v4u bf[BD][4];
#pragma unroll
    for (int kq = 0; kq < BD - 1; ++kq)
#pragma unroll
      for (int mt = 0; mt < 4; ++mt) bf[kq][mt] = *(LAS v4u*)(bl + (kq * 4 + mt) * 1024);
    static_for<0, 20>([&](auto kc) {
      constexpr int kk = kc, cu = kk % BD, nx = (kk + BD - 1) % BD;
      if constexpr (kk + PF < 20) {
        constexpr int f = (kk + PF) * 2;
        gld16s_sc1<(f & 3) * 1024>(wa[kk + PF][0], wl, wp + (f >> 2) * 4096);
        gld16s_sc1<((f + 1) & 3) * 1024>(wa[kk + PF][1], wl, wp + ((f + 1) >> 2) * 4096);
      }
      constexpr int younger = 2 * ((kk + PF < 20 ? kk + PF : 19) - kk);
      wait_vm<younger>(wa[kk][0], wa[kk][1]);
      if constexpr (kk == 9 - BD + 1 || kk == 16 - BD + 1) *(volatile LAS unsigned*)(sy + 4 * ((kk == 9 - BD + 1 ? SY_KA : SY_KB) + wave)) = 1u;
      if constexpr (kk + BD - 1 < 20) {
#pragma unroll
        for (int mt = 0; mt < 4; ++mt) bf[nx][mt] = *(LAS v4u*)(bl + ((kk + BD - 1) * 4 + mt) * 1024);
      }
#pragma unroll
      for (int mt = 0; mt < 4; ++mt) {
        acc[mt][0] = mfma16(wa[kk][0], bf[cu][mt], acc[mt][0]);
        acc[mt][1] = mfma16(wa[kk][1], bf[cu][mt], acc[mt][1]);
      }
      if constexpr (kk + BD - 1 < 20) {
#pragma unroll
        for (int i = 0; i < 4; ++i) { SGB(SG_MFMA, 1); SGB(SG_DS_R, 1); }
        SGB(SG_MFMA, 4);
      }
      SCHED_FENCE();
    });
    CSTAMP(0, 13);
    lds_wait(sy, SY_R3, 8);
    const float bb[8] = {bf_lo(bias.x), bf_hi(bias.x), bf_lo(bias.y), bf_hi(bias.y), bf_lo(bias.z), bf_hi(bias.z), bf_lo(bias.w), bf_hi(bias.w)};
#pragma unroll
    for (int mt = 0; mt < 4; ++mt) {
      const float r3 = *(LAS float*)(lds + K2_R3_OFF + 4 * (16 * mt + (fresh_lane() & 15)));
      const f32x2 r32 = {r3, r3};
      v4u hp;
#pragma unroll
      for (int q = 0; q < 4; ++q) {
        const f32x2 a = {acc[mt][q >> 1][2 * (q & 1)], acc[mt][q >> 1][2 * (q & 1) + 1]};
        const f32x2 h = gelu_tanh2(__builtin_elementwise_fma(a, r32, f32x2{bb[2 * q], bb[2 * q + 1]}));
        hp[q] = pk_bf16(h[0], h[1]);
      }
      __builtin_amdgcn_raw_buffer_store_b128(hp,
                                             hrs, (((4 * rt + mt) * 54 + P) * 64 + lane) * 16, 0, 16);
    }
  }
  WAIT_VM(0);
  BARRIER();
  CSTAMP(0, 14);
  if (tid == 0) flag_store(ws + WS_F2, 8 * rt + cg8, tag);
  const int ct = cg8;
  LAS float* ep = (LAS float*)(lds + K3_EPI_OFF);
  if (wave >= 4) {
    v4u park[30];
    w2park(wave + 2, park, 0);
    lds_wait4(sy, SY_W2FREE + 4 * (wave - 3));
    park_dump(park, wave == 4 ? k3_slot(6) : wave == 5 ? k3_slot(7) : k3_slot(8), true);
    lds_signal(sy, SY_W2READY + wave + 2, lane);
  } else {
    if (wave == 0) { poll_flags<8, SL2>(ws + WS_F2 + 8 * 128 * rt, tag, lane); lds_signal(sy, SY_HOK, lane); }
    else if (wave < 3) {
      const int i = tid - 64;
      if (i < 80) {
        const int f = i < 64 ? 64 * ct + i : 512 + 16 * ct + i - 64;
        float gsum = 0.f;
#pragma unroll
        for (int kq = 0; kq < 4; ++kq) gsum += __uint_as_float(__hip_atomic_load((gu32*)mod + kq * 3840 + 5 * D_ + f, __ATOMIC_RELAXED, __HIP_MEMORY_SCOPE_AGENT));
        ep[i] = rnd_bf16(gsum);
        ep[80 + i] = bf_lo((unsigned)b2[f]);
      }
      WAIT_LGKM0();
    }
    const __amdgpu_buffer_rsrc_t outr = rsrc_of(out, S_ * D_ * 2);
    unsigned oo[3];
#pragma unroll
    for (int j = 0; j < 3; ++j) {
      const int c = 64 * j + (tid & 63), rr = c / 10, cc = c - 10 * rr;
      oo[j] = c < 160 ? ((64 * rt + 16 * wave + rr) * D_ + (cc < 8 ? 64 * ct + 8 * cc : 512 + 16 * ct + 8 * (cc - 8))) * 2 : 0x7ffffff0u;
    }
    asm volatile("" : "+v"(oo[0]), "+v"(oo[1]), "+v"(oo[2]));
    lds_wait(sy, SY_HOK, 1);
    CSTAMP(0, 15);
    const int lane = fresh_lane();
    const char* hp0 = (const char*)Ht + (size_t)(4 * rt) * 54 * 1024;
    const unsigned hl = lane * 16;
    const int n = lane & 15, g = lane >> 4;
    const __amdgpu_buffer_rsrc_t x2r = __builtin_amdgcn_make_buffer_rsrc(x2g, 0, S_ * D_ * 2, 0x00020000);
    const int mt_f = wave;
    const int rg8 = 8 * rt + 2 * mt_f + (n >> 3);
    v2u xr[5];
#pragma unroll
    for (int t = 0; t < 5; ++t) {
      const int chunk = (t < 4 ? 8 * ct + 2 * t : 64 + 2 * ct) + (g >> 1);
      xr[t] = __builtin_amdgcn_raw_buffer_load_b64(x2r, ((rg8 * 10 + (chunk >> 3)) * 64 + (n & 7) + 8 * (chunk & 7)) * 16 + 8 * (g & 1), 0, 16);
    }
    SCHED_FENCE();
    f32x4 fin[5], gte[5], bia[5];
    auto g2loop = [&](auto kqc) {
      constexpr int KQ = kqc, NS = KQ < 2 ? 14 : 13, NF = 4 * NS;
      f32x4 acc[4][5];
#pragma unroll
      for (int mt = 0; mt < 4; ++mt)
#pragma unroll
        for (int t = 0; t < 5; ++t) acc[mt][t] = f32x4{0.f, 0.f, 0.f, 0.f};
      v4u hb[8];
      unsigned hv = hl + KQ * 1024;
      const char* hm[4] = {hp0, hp0 + 54 * 1024, hp0 + 2 * 54 * 1024, hp0 + 3 * 54 * 1024};
      auto hload = [&](auto fc) {
        constexpr int f = fc;
        if constexpr (f < NF) gld16s_sc1<0>(hb[f & 7], hv + (f >> 2) * 4096, hm[f & 3]);
      };
      static_for<0, 7>(hload);
      const int q4 = n >> 2, p4 = n & 3;
      const unsigned ab = l0 + (8 * g + q4) * 128 + (((q4 >> 1) | ((g & 1) << 1)) << 5) + 8 * p4, at = l0 + 4096 + lane * 16;
      auto wfrag = [&](auto kc, int t) {
        constexpr int k = kc, off = k3_slot(k / 6) + (k % 6) * 5120;
        if (t < 4) { LAS char* a = (LAS char*)(size_t)(ab ^ (t << 5)) + off; return tr_read2(a, a + 512); }
        return *(LAS v4u*)((LAS char*)(size_t)at + off);
      };
      v4u wb[2][5];
#pragma unroll
      for (int t = 0; t < 5; ++t) wb[0][t] = wfrag(std::integral_constant<int, KQ>{}, t);
      unsigned rdy = 1;
      static_for<0, NS>([&](auto ic) {
        constexpr int i = ic, cu = i & 1, nx = cu ^ 1, k = 4 * i + KQ;
        constexpr bool more = i + 1 < NS;
        constexpr int st = k / 6, sn = (k + 4) / 6, s2 = (k + 8) / 6;
        using KN = std::integral_constant<int, more ? k + 4 : k>;
        if constexpr (more && sn != st && sn >= 2) { if (__builtin_amdgcn_readfirstlane(rdy) == 0) lds_wait(sy, SY_W2READY + sn, 1); }
        SCHED_FENCE();
        static_for<0, 4>([&](auto mc) {
          constexpr int mt = mc, f = 4 * i + mt;
          wait_vm<(f + 6 < NF ? 6 : NF - 1 - f) - (f + 6 < NF ? 0 : 0)>(hb[f & 7]);
          acc[mt][0] = mfma16(wb[cu][0], hb[f & 7], acc[mt][0]);
          SCHED_FENCE();
          hload(std::integral_constant<int, f + 7>{});
          if constexpr (more) {
            if constexpr (mt == 0) {
              if constexpr (sn != st && st < 4) *(volatile LAS unsigned*)(sy + 4 * (SY_W2FREE + 4 * st + KQ)) = 1u;
              if constexpr (i + 2 < NS && s2 != sn && s2 >= 2) rdy = lds_peek(sy, SY_W2READY + s2);
              wb[nx][0] = wfrag(KN{}, 0);
            } else if constexpr (mt == 1) { wb[nx][1] = wfrag(KN{}, 1); wb[nx][2] = wfrag(KN{}, 2); }
            else if constexpr (mt == 2) { wb[nx][3] = wfrag(KN{}, 3); wb[nx][4] = wfrag(KN{}, 4); }
          }
#pragma unroll
          for (int t = 1; t < 5; ++t) acc[mt][t] = mfma16(wb[cu][t], hb[f & 7], acc[mt][t]);
          if constexpr (more && mt < 3) {
#pragma unroll
            for (int q = 0; q < 4; ++q) { SGB(SG_DS_R, 1); SGB(SG_MFMA, 1); }
          }
          SCHED_FENCE();
        });
      });
      CSTAMP(0, 16);
#pragma unroll
      for (int t = 0; t < 5; ++t) { gte[t] = *(LAS f32x4*)(ep + 16 * t + 4 * g); bia[t] = *(LAS f32x4*)(ep + 80 + 16 * t + 4 * g); }
      BARRIER();
      LAS char* xb = lds + k3_slot(0) + lane * 16;
#pragma unroll
      for (int mt = 0; mt < 4; ++mt) {
        if (mt != KQ) {
#pragma unroll
          for (int t = 0; t < 5; ++t) *(LAS f32x4*)(xb + (mt * 3 + (KQ > mt ? KQ - 1 : KQ)) * 5120 + t * 1024) = acc[mt][t];
        }
      }
      WAIT_LGKM0();
      BARRIER();
#pragma unroll
      for (int t = 0; t < 5; ++t) {
        LAS char* xi = xb + KQ * 3 * 5120 + t * 1024;
        fin[t] = (acc[KQ][t] + *(LAS f32x4*)(xi)) + (*(LAS f32x4*)(xi + 5120) + *(LAS f32x4*)(xi + 2 * 5120));
      }
    };
    if (wave == 0) g2loop(std::integral_constant<int, 0>{});
    else if (wave == 1) g2loop(std::integral_constant<int, 1>{});
    else if (wave == 2) g2loop(std::integral_constant<int, 2>{});
    else g2loop(std::integral_constant<int, 3>{});
    LAS char* ot = lds + wave * 2560;
#pragma unroll
    for (int t = 0; t < 5; ++t) {
      const float o0 = fmaf(gte[t][0], fin[t][0] + bia[t][0], bf_lo(xr[t].x)), o1 = fmaf(gte[t][1], fin[t][1] + bia[t][1], bf_hi(xr[t].x));
      const float o2 = fmaf(gte[t][2], fin[t][2] + bia[t][2], bf_lo(xr[t].y)), o3 = fmaf(gte[t][3], fin[t][3] + bia[t][3], bf_hi(xr[t].y));
      *(LAS v2u*)(ot + n * 160 + 32 * t + 8 * g) = v2u{pk_bf16(o0, o1), pk_bf16(o2, o3)};
    }
    v4u ov[3];
#pragma unroll
    for (int j = 0; j < 3; ++j) ov[j] = *(LAS v4u*)(ot + 16 * (j < 2 || lane < 32 ? 64 * j + lane : lane));
#pragma unroll
    for (int j = 0; j < 3; ++j) __builtin_amdgcn_raw_buffer_store_b128(ov[j], outr, oo[j], 0, 0);
  }
  STAMP(0, 1);
  STAMP_FLUSH(0);
}

extern "C" void kernel_launch(void* const* d_in, const int* in_sizes, int n_in, void* d_out, int out_size,
                              void* d_ws, size_t ws_size, hipStream_t stream) {
  char* ws = (char*)d_ws;
  (void)hipFuncSetAttribute((const void*)adaln_block, hipFuncAttributeMaxDynamicSharedMemorySize, K23_LDS);
  adaln_block<<<256, 512, K23_LDS, stream>>>((const u16*)d_in[0], (const u16*)d_in[1], (const u16*)d_in[2], (const u16*)d_in[3],
                                             (const u16*)d_in[4], (const u16*)d_in[5], (const u16*)d_in[7], (const u16*)d_in[8],
                                             (const u16*)d_in[9], (const u16*)d_in[10], (const u16*)d_in[11], (const u16*)d_in[12],
                                             (u16*)d_out, ws STAMP_PASS);
}
```
